```python
import torch

def _softmax(x, axis=-1):
    return torch.softmax(x, dim=axis)

def _gelu(x):
    return torch.nn.functional.gelu(x, approximate='tanh')

def _silu(x):
    return torch.nn.functional.silu(x)

def _sigmoid(x):
    return torch.sigmoid(x)

def _layernorm(x, axis=-1, eps=1e-05):
    m = x.mean(dim=axis, keepdim=True)
    v = x.var(dim=axis, keepdim=True, unbiased=False)
    return (x - m) / torch.sqrt(v + eps)

def _scatter_add(tgt, idx, src, axis=0):
    return tgt.clone().index_add_(axis, idx, src)

def _segment_sum(src, seg, n):
    out = torch.zeros((n,) + src.shape[1:], dtype=src.dtype)
    return out.index_add_(0, seg, src)

def _pad(x, pw):
    flat = tuple((v for pair in reversed(pw) for v in pair))
    return torch.nn.functional.pad(x, flat)
kernel_name = 'interleave_layers__ssm_conv_h3__memory_aug_recurrent_mem_tr__prompt'

def setup_inputs():

    _w = lambda a: (lambda t: t.to(torch.bfloat16) if t.is_floating_point() else t)(torch.as_tensor(a))

    def _A_setup():
        B, S, D = (1, 2048, 448)
        ks = 4
        return {'q': _w((lambda *s: torch.randn(*s, dtype=torch.bfloat16) * 0.05)(B, S, D)), 'k': _w((lambda *s: torch.randn(*s, dtype=torch.bfloat16) * 0.2)(B, S, D)), 'v': _w((lambda *s: torch.randn(*s, dtype=torch.bfloat16) * 0.2)(B, S, D)), 'shift_k': _w((lambda *s: torch.randn(*s, dtype=torch.bfloat16) * 0.2)(D, ks) * 0.5), 'ssm_k': _w((lambda *s: torch.randn(*s, dtype=torch.bfloat16) * 0.2)(D, S) * (1.0 / S ** 0.5)), 'ssm_D': _w((lambda *s: torch.randn(*s, dtype=torch.bfloat16) * 0.2)(D))}
    _A_in = _A_setup()

    def _B_setup():
        B, S, Dm = (1, 2048, 448)
        H, Dh = (16, 256)
        M, seg = (8, 32)
        seg = min(seg, S)
        S = S // seg * seg
        return {'x': _w((lambda *s: torch.randn(*s, dtype=torch.bfloat16) * 0.05)(B, S, Dm)), 'mem0': _w((lambda *s: torch.randn(*s, dtype=torch.bfloat16) * 0.2)(B, M, Dm)), 'Wq': _w((lambda *s: torch.randn(*s, dtype=torch.bfloat16) * 1.6)(Dm, H, Dh) * Dm ** (-0.5)), 'Wk': _w((lambda *s: torch.randn(*s, dtype=torch.bfloat16) * 1.6)(Dm, H, Dh) * Dm ** (-0.5)), 'Wv': _w((lambda *s: torch.randn(*s, dtype=torch.bfloat16) * 0.2)(Dm, H, Dh) * Dm ** (-0.5)), 'Wo': _w((lambda *s: torch.randn(*s, dtype=torch.bfloat16) * 0.2)(H, Dh, Dm) * (H * Dh) ** (-0.5)), 'seg_len': seg}
    _B_in = _B_setup()
    return {'A_q': _A_in['q'], 'A_k': _A_in['k'], 'A_v': _A_in['v'], 'A_shift_k': _A_in['shift_k'], 'A_ssm_k': _A_in['ssm_k'], 'A_ssm_D': _A_in['ssm_D'], 'B_x': _B_in['x'], 'B_mem0': _B_in['mem0'], 'B_Wq': _B_in['Wq'], 'B_Wk': _B_in['Wk'], 'B_Wv': _B_in['Wv'], 'B_Wo': _B_in['Wo'], 'B_seg_len': _B_in['seg_len']}

def reference(A_q, A_k, A_v, A_shift_k, A_ssm_k, A_ssm_D, B_x, B_mem0, B_Wq, B_Wk, B_Wv, B_Wo, B_seg_len):

    def _A_ref(q, k, v, shift_k, ssm_k, ssm_D):
        B, S, D = q.shape
        ks = shift_k.shape[-1]
        k_pad = _pad(k, ((0, 0), (ks - 1, 0), (0, 0)))
        k_shift = torch.zeros_like(k)
        for t in range(ks):
            k_shift = k_shift + k_pad[:, t:t + S, :] * shift_k[:, ks - 1 - t][None, None, :]
        kv = k_shift * v
        L = 2 * S
        kv_pad = _pad(kv, ((0, 0), (0, S), (0, 0)))
        kf_pad = _pad(ssm_k, ((0, 0), (0, S)))
        kv_f = (lambda x, axes=(-2, -1): torch.fft.rfft2(x.float(), dim=axes))(kv_pad[:, None, :, :], axes=(1, 2))
        kf_f = (lambda x, axes=(-2, -1): torch.fft.rfft2(x.float(), dim=axes))(kf_pad[:, None, :], axes=(1, 2))
        kf_f = (lambda x, *d: x.permute(*d))(kf_f, 1, 2, 0)
        y_f = kv_f * kf_f[None, :, :, :]
        y = (lambda x, s, axes=(-2, -1): torch.fft.irfft2(x, s=s, dim=axes).to(torch.bfloat16))(y_f, (1, L), axes=(1, 2))[:, 0, :S, :]
        y = y + kv * ssm_D[None, None, :]
        return q * y

    def _B_ref(x, mem0, Wq, Wk, Wv, Wo, seg_len):
        B, S, Dm = x.shape
        H, Dh = (Wq.shape[1], Wq.shape[2])
        M = mem0.shape[1]
        scale = Dh ** (-0.5)
        n_seg = S // seg_len
        L = M + seg_len
        idx = torch.arange(L)
        mask = (idx[:, None] < M) | (idx[:, None] >= idx[None, :])
        mem = mem0
        outs = []
        for s in range(n_seg):
            xs = x[:, s * seg_len:(s + 1) * seg_len, :].clone()
            h = torch.concatenate([mem, xs], axis=1)
            q = torch.einsum('btd,dhe->bhte', h, Wq)
            k = torch.einsum('btd,dhe->bhte', h, Wk)
            v = torch.einsum('btd,dhe->bhte', h, Wv)
            scores = torch.einsum('bhqe,bhke->bhqk', q, k) * scale
            scores = torch.where(mask[None, None], scores, -1000000000.0)
            p = _softmax(scores, axis=-1)
            o = torch.einsum('bhqk,bhke->bhqe', p, v)
            o = torch.einsum('bhte,hed->btd', o, Wo)
            mem = o[:, :M, :].clone()
            outs.append(o[:, M:, :])
        return torch.concatenate(outs, axis=1)
    x = A_q
    x = x + _A_ref(_layernorm(x, axis=-1), A_k, A_v, A_shift_k, A_ssm_k, A_ssm_D)
    x = x + _B_ref(_layernorm(x, axis=-1), B_mem0, B_Wq, B_Wk, B_Wv, B_Wo, B_seg_len)
    x = x + _A_ref(_layernorm(x, axis=-1), A_k, A_v, A_shift_k, A_ssm_k, A_ssm_D)
    x = x + _B_ref(_layernorm(x, axis=-1), B_mem0, B_Wq, B_Wk, B_Wv, B_Wo, B_seg_len)
    return x
```

```cpp
#include <hip/hip_runtime.h>
#include <hip/hip_bf16.h>
#include <stdint.h>

constexpr int S_ = 2048, D_ = 448, H_ = 16, DH_ = 256, NH_ = 4096, SEG_ = 32, NSEG_ = 64, MM_ = 8;

typedef unsigned short u16;
typedef __attribute__((ext_vector_type(8))) short bf16x8;
typedef __attribute__((ext_vector_type(4))) float f32x4;
typedef __attribute__((ext_vector_type(16))) float f32x16;
typedef unsigned v4u __attribute__((ext_vector_type(4)));
typedef unsigned v2u __attribute__((ext_vector_type(2)));

#define DI __device__ __forceinline__
#define MFMA32(a, b, c) __builtin_amdgcn_mfma_f32_32x32x16_bf16((a), (b), (c), 0, 0, 0)
#define MFMA16(a, b, c) __builtin_amdgcn_mfma_f32_16x16x32_bf16((a), (b), (c), 0, 0, 0)

DI float bf2f(u16 v) { return __uint_as_float(((unsigned)v) << 16); }
typedef __attribute__((ext_vector_type(2))) __bf16 bf16x2_t;
typedef __attribute__((ext_vector_type(2))) float f32x2;
DI unsigned pack2(float a, float b) { f32x2 f = {a, b}; return __builtin_bit_cast(unsigned, __builtin_convertvector(f, bf16x2_t)); }
DI u16 f2bf(float f) { return (u16)(pack2(f, 0.f) & 0xffffu); }
DI float rb(float f) { return __uint_as_float(pack2(f, 0.f) << 16); }

constexpr size_t al(size_t x) { return (x + 255) & ~(size_t)255; }
constexpr size_t OFF_WQKVT = 0;
constexpr size_t OFF_WOT   = OFF_WQKVT + al((size_t)12288 * 448 * 2);
constexpr size_t OFF_KVT   = OFF_WOT + al((size_t)448 * 4096 * 2);
constexpr size_t OFF_YT    = OFF_KVT + al((size_t)448 * 2048 * 2);
constexpr size_t OFF_X1    = OFF_YT + al((size_t)448 * 2048 * 2);
constexpr size_t OFF_X3    = OFF_X1 + al((size_t)2048 * 448 * 2);
constexpr size_t OFF_HB    = OFF_X3 + al((size_t)2048 * 448 * 2);
constexpr size_t OFF_Q     = OFF_HB + al((size_t)2 * 2048 * 448 * 2);
constexpr size_t OFF_K     = OFF_Q + al((size_t)2 * 2048 * 4096 * 2);
constexpr size_t OFF_VT    = OFF_K + al((size_t)2 * 2048 * 4096 * 2);
constexpr size_t OFF_KMEM  = OFF_VT + al((size_t)2 * 2048 * 4096 * 2);
constexpr size_t OFF_VMEMT = OFF_KMEM + al((size_t)2 * 64 * 8 * 4096 * 2);
constexpr size_t OFF_PART  = OFF_VMEMT + al((size_t)2 * 64 * 8 * 4096 * 2);
constexpr int RING = 4, NCH = 64, GG = 4;
constexpr int XA_UNITS = 320 + 128;
constexpr size_t OFF_XA    = OFF_PART + al((size_t)16 * 2048 * 448 * 4);
constexpr size_t SZ_XA     = (size_t)RING * NCH * GG * XA_UNITS * 16;
constexpr size_t OFF_XB    = OFF_XA + al(SZ_XA);
constexpr size_t SZ_XB     = (size_t)RING * NCH * 16 * 32 * 16;
constexpr size_t OFF_XC    = OFF_XB + al(SZ_XB);
constexpr size_t SZ_XC     = (size_t)RING * NCH * 896 * 16;
constexpr size_t OFF_ERR   = OFF_XC + al(SZ_XC);
constexpr size_t WS_END    = OFF_ERR + 256;

__device__ void transpose_tile(const u16* __restrict__ src, int C, int r0, int c0, u16* __restrict__ dst, int ldd, int dr0, u16 (*tile)[66]) {
    const int tid = threadIdx.x;
    for (int i = tid; i < 64 * 8; i += 256) {
        int r = i >> 3, cq = i & 7;
        v4u v = *(const v4u*)(src + (size_t)(r0 + r) * C + c0 + cq * 8);
        unsigned w[4] = {v.x, v.y, v.z, v.w};
#pragma unroll
        for (int j = 0; j < 4; ++j) { tile[r][cq * 8 + 2 * j] = (u16)(w[j] & 0xffff); tile[r][cq * 8 + 2 * j + 1] = (u16)(w[j] >> 16); }
    }
    __syncthreads();
    for (int i = tid; i < 64 * 8; i += 256) {
        int c = i >> 3, rq = i & 7;
        unsigned w[4];
#pragma unroll
        for (int j = 0; j < 4; ++j) w[j] = (unsigned)tile[rq * 8 + 2 * j][c] | ((unsigned)tile[rq * 8 + 2 * j + 1][c] << 16);
        v4u v = {w[0], w[1], w[2], w[3]};
        *(v4u*)(dst + (size_t)(dr0 + c0 + c) * ldd + r0 + rq * 8) = v;
    }
    __syncthreads();
}

__global__ void __launch_bounds__(256) k_prep(const u16* __restrict__ Wq, const u16* __restrict__ Wk, const u16* __restrict__ Wv, const u16* __restrict__ Wo,
                                               const u16* __restrict__ kin, const u16* __restrict__ vin, const u16* __restrict__ shk, char* __restrict__ ws) {
    __shared__ u16 tile[64][66];
    int b = blockIdx.x;
    if (b < 3 * 448) {
        int w = b / 448, t = b % 448;
        const u16* src = w == 0 ? Wq : (w == 1 ? Wk : Wv);
        int r0 = (t / 64) * 64, c0 = (t % 64) * 64;
        transpose_tile(src, 4096, r0, c0, (u16*)(ws + OFF_WQKVT), 448, w * 4096, tile);
        return;
    }
    b -= 3 * 448;
    if (b < 448) {
        int r0 = (b / 7) * 64, c0 = (b % 7) * 64;
        transpose_tile(Wo, 448, r0, c0, (u16*)(ws + OFF_WOT), 4096, 0, tile);
        return;
    }
    b -= 448;
    {
        int t0 = (b / 7) * 64, d0 = (b % 7) * 64;
        const int tid = threadIdx.x;
        for (int i = tid; i < 64 * 64; i += 256) {
            int tt = i >> 6, dd = i & 63;
            int t = t0 + tt, d = d0 + dd;
            float acc = 0.f;
#pragma unroll
            for (int j = 0; j < 4; ++j) {
                int ts = t + j - 3;
                float kv_ = ts >= 0 ? bf2f(kin[(size_t)ts * D_ + d]) : 0.f;
                float pr = rb(kv_ * bf2f(shk[d * 4 + (3 - j)]));
                acc = rb(acc + pr);
            }
            float kvv = rb(acc * bf2f(vin[(size_t)t * D_ + d]));
            tile[dd][tt] = f2bf(kvv);
        }
        __syncthreads();
        u16* kvT = (u16*)(ws + OFF_KVT);
        for (int i = tid; i < 64 * 32; i += 256) {
            int dd = i >> 5, tp = i & 31;
            unsigned w = (unsigned)tile[dd][2 * tp] | ((unsigned)tile[dd][2 * tp + 1] << 16);
            *(unsigned*)(kvT + (size_t)(d0 + dd) * S_ + t0 + 2 * tp) = w;
        }
    }
}

constexpr int CV_GS = 2088;
__global__ void __launch_bounds__(256) k_conv(const u16* __restrict__ ssmk, const u16* __restrict__ ssmD, char* __restrict__ ws) {
    __shared__ __attribute__((aligned(16))) u16 gcp[8 * CV_GS];
    __shared__ __attribute__((aligned(16))) u16 kvs[96 * 40];
    __shared__ float ysum[2048];
    const int d = blockIdx.x, tid = threadIdx.x, lane = tid & 63, wave = tid >> 6;
    const u16* hrow = ssmk + (size_t)d * S_;
    const u16* kvT = (const u16*)(ws + OFF_KVT) + (size_t)d * S_;
    for (int i = tid; i < 8 * CV_GS; i += 256) {
        int p = i / CV_GS, x = i % CV_GS;
        int m = x + p;
        u16 v = 0;
        if (m <= 2047) v = hrow[2047 - m];
        gcp[i] = v;
    }
    for (int i = tid; i < 96 * 40; i += 256) {
        int blk = i / 40, j = i % 40;
        u16 v = 0;
        if (blk >= 32 && j < 32) v = kvT[(blk - 32) * 32 + j];
        kvs[i] = v;
    }
    for (int i = tid; i < 2048; i += 256) ysum[i] = 0.f;
    __syncthreads();
    f32x16 acc0, acc1;
#pragma unroll
    for (int i = 0; i < 16; ++i) { acc0[i] = 0.f; acc1[i] = 0.f; }
    const int r = lane & 31, hh = lane >> 5;
    for (int dl = wave; dl < 64; dl += 4) {
#pragma unroll
        for (int ks = 0; ks < 2; ++ks) {
            int a = 32 * dl + r - 16 * ks - 8 * hh;
            int base = 2047 - a;
            int p = base & 7, q = base >> 3;
            bf16x8 af = *(const bf16x8*)(gcp + p * CV_GS + q * 8);
            {
                int blk = 32 + (32 + r - dl);
                bf16x8 bfrag = *(const bf16x8*)(kvs + blk * 40 + 16 * ks + 8 * hh);
                acc1 = MFMA32(af, bfrag, acc1);
            }
            if (dl < 32) {
                int blk = 32 + (r - dl);
                bf16x8 bfrag = *(const bf16x8*)(kvs + blk * 40 + 16 * ks + 8 * hh);
                acc0 = MFMA32(af, bfrag, acc0);
            }
        }
    }
#pragma unroll
    for (int reg = 0; reg < 16; ++reg) {
        int j = (reg & 3) + 8 * (reg >> 2) + 4 * hh;
        atomicAdd(&ysum[32 * r + j], acc0[reg]);
        atomicAdd(&ysum[32 * (32 + r) + j], acc1[reg]);
    }
    __syncthreads();
    const float Dd = bf2f(ssmD[d]);
    u16* yT = (u16*)(ws + OFF_YT) + (size_t)d * S_;
    for (int t = tid; t < 2048; t += 256) {
        float kvv = bf2f(kvs[(32 + (t >> 5)) * 40 + (t & 31)]);
        float y = rb(rb(ysum[t]) + rb(kvv * Dd));
        yT[t] = f2bf(y);
    }
}

DI float wave_sum(float v) {
#pragma unroll
    for (int m = 32; m >= 1; m >>= 1) v += __shfl_xor(v, m);
    return v;
}
DI void layernorm7(const float (&x)[7], float (&o)[7]) {
    float s = 0.f;
#pragma unroll
    for (int i = 0; i < 7; ++i) s += x[i];
    s = wave_sum(s);
    const float mean = s * (1.0f / 448.0f);
    float q = 0.f;
#pragma unroll
    for (int i = 0; i < 7; ++i) { float dlt = x[i] - mean; q += dlt * dlt; }
    q = wave_sum(q);
    const float m = rb(mean), v = rb(q * (1.0f / 448.0f));
    const float den = rb(sqrtf(rb(v + 1e-5f)));
#pragma unroll
    for (int i = 0; i < 7; ++i) o[i] = rb(rb(x[i] - m) / den);
}

__global__ void __launch_bounds__(256) k_stage1(const u16* __restrict__ x0, char* __restrict__ ws) {
    __shared__ u16 yt[32][450];
    const int t0 = blockIdx.x * 32, tid = threadIdx.x, lane = tid & 63, wave = tid >> 6;
    const u16* yT = (const u16*)(ws + OFF_YT);
    for (int i = tid; i < 448 * 16; i += 256) {
        int d = i >> 4, tp = i & 15;
        unsigned w = *(const unsigned*)(yT + (size_t)d * S_ + t0 + 2 * tp);
        yt[2 * tp][d] = (u16)(w & 0xffff); yt[2 * tp + 1][d] = (u16)(w >> 16);
    }
    __syncthreads();
    u16* x1 = (u16*)(ws + OFF_X1); u16* hb = (u16*)(ws + OFF_HB);
    for (int rr = wave; rr < 32; rr += 4) {
        int t = t0 + rr;
        float x[7], ln[7];
#pragma unroll
        for (int i = 0; i < 7; ++i) x[i] = bf2f(x0[(size_t)t * D_ + lane + 64 * i]);
        layernorm7(x, ln);
#pragma unroll
        for (int i = 0; i < 7; ++i) x[i] = rb(x[i] + rb(ln[i] * bf2f(yt[rr][lane + 64 * i])));
        layernorm7(x, ln);
#pragma unroll
        for (int i = 0; i < 7; ++i) { x1[(size_t)t * D_ + lane + 64 * i] = f2bf(x[i]); hb[(size_t)t * D_ + lane + 64 * i] = f2bf(ln[i]); }
    }
}

constexpr int GS = 72;
__global__ void __launch_bounds__(256) k_qkv(const char* __restrict__ ws_c, char* __restrict__ ws, int layer) {
    __shared__ __attribute__((aligned(16))) u16 sW[128 * GS];
    __shared__ __attribute__((aligned(16))) u16 sH[128 * GS];
    const int tid = threadIdx.x, lane = tid & 63, wave = tid >> 6;
    const int nt = blockIdx.x % 96, tt = blockIdx.x / 96;
    const int n0 = nt * 128, t0 = tt * 128;
    const u16* WT = (const u16*)(ws_c + OFF_WQKVT) + (size_t)n0 * D_;
    const u16* HB = (const u16*)(ws_c + OFF_HB) + (size_t)layer * S_ * D_ + (size_t)t0 * D_;
    const bool isV = n0 >= 8192;
    const int wr = wave >> 1, wc = wave & 1;
    const int r = lane & 31, hh = lane >> 5;
    f32x16 acc[2][2];
#pragma unroll
    for (int a = 0; a < 2; ++a)
#pragma unroll
        for (int b = 0; b < 2; ++b)
#pragma unroll
            for (int i = 0; i < 16; ++i) acc[a][b][i] = 0.f;
    v4u pw[4], ph[4];
#pragma unroll
    for (int i = 0; i < 4; ++i) {
        int c = tid + 256 * i, row = c >> 3, kq = c & 7;
        pw[i] = *(const v4u*)(WT + (size_t)row * D_ + kq * 8);
        ph[i] = *(const v4u*)(HB + (size_t)row * D_ + kq * 8);
    }
    for (int kb = 0; kb < 7; ++kb) {
        __syncthreads();
#pragma unroll
        for (int i = 0; i < 4; ++i) {
            int c = tid + 256 * i, row = c >> 3, kq = c & 7;
            *(v4u*)(sW + row * GS + kq * 8) = pw[i];
            *(v4u*)(sH + row * GS + kq * 8) = ph[i];
        }
        __syncthreads();
        if (kb < 6) {
#pragma unroll
            for (int i = 0; i < 4; ++i) {
                int c = tid + 256 * i, row = c >> 3, kq = c & 7;
                pw[i] = *(const v4u*)(WT + (size_t)row * D_ + (kb + 1) * 64 + kq * 8);
                ph[i] = *(const v4u*)(HB + (size_t)row * D_ + (kb + 1) * 64 + kq * 8);
            }
        }
#pragma unroll
        for (int ks = 0; ks < 4; ++ks) {
            bf16x8 fw[2], fh[2];
#pragma unroll
            for (int a = 0; a < 2; ++a) {
                fw[a] = *(const bf16x8*)(sW + (wr * 64 + a * 32 + r) * GS + ks * 16 + hh * 8);
                fh[a] = *(const bf16x8*)(sH + (wc * 64 + a * 32 + r) * GS + ks * 16 + hh * 8);
            }
            if (!isV) {
#pragma unroll
                for (int a = 0; a < 2; ++a)
#pragma unroll
                    for (int b = 0; b < 2; ++b) acc[a][b] = MFMA32(fw[a], fh[b], acc[a][b]);
            } else {
#pragma unroll
                for (int a = 0; a < 2; ++a)
#pragma unroll
                    for (int b = 0; b < 2; ++b) acc[a][b] = MFMA32(fh[b], fw[a], acc[a][b]);
            }
        }
    }
    if (!isV) {
        u16* dst = (u16*)(ws + (n0 < 4096 ? OFF_Q : OFF_K)) + (size_t)layer * S_ * NH_;
        const int nb = n0 & 4095;
#pragma unroll
        for (int a = 0; a < 2; ++a)
#pragma unroll
            for (int b = 0; b < 2; ++b) {
                int t = t0 + wc * 64 + b * 32 + r;
#pragma unroll
                for (int g4 = 0; g4 < 4; ++g4) {
                    int n = nb + wr * 64 + a * 32 + 8 * g4 + 4 * hh;
                    v2u v = {pack2(acc[a][b][4 * g4], acc[a][b][4 * g4 + 1]), pack2(acc[a][b][4 * g4 + 2], acc[a][b][4 * g4 + 3])};
                    *(v2u*)(dst + (size_t)t * NH_ + n) = v;
                }
            }
    } else {
        u16* dst = (u16*)(ws + OFF_VT) + (size_t)layer * S_ * NH_;
        const int nb = n0 - 8192;
#pragma unroll
        for (int a = 0; a < 2; ++a)
#pragma unroll
            for (int b = 0; b < 2; ++b) {
                int n = nb + wr * 64 + a * 32 + r;
#pragma unroll
                for (int g4 = 0; g4 < 4; ++g4) {
                    int t = t0 + wc * 64 + b * 32 + 8 * g4 + 4 * hh;
                    v2u v = {pack2(acc[a][b][4 * g4], acc[a][b][4 * g4 + 1]), pack2(acc[a][b][4 * g4 + 2], acc[a][b][4 * g4 + 3])};
                    *(v2u*)(dst + (size_t)n * S_ + t) = v;
                }
            }
    }
}

constexpr int OS = 264;
__global__ void __launch_bounds__(256) k_att_out(const char* __restrict__ ws_c, char* __restrict__ ws, int layer) {
    __shared__ __attribute__((aligned(16))) u16 so[128 * OS];
    const int tid = threadIdx.x, lane = tid & 63, wave = tid >> 6;
    const int h = blockIdx.x & 15, grp = blockIdx.x >> 4;
    const int seg = grp * 4 + wave, t0 = seg * SEG_;
    const int r = lane & 31, hh = lane >> 5;
    const u16* Q = (const u16*)(ws_c + OFF_Q) + (size_t)layer * S_ * NH_;
    const u16* K = (const u16*)(ws_c + OFF_K) + (size_t)layer * S_ * NH_;
    const u16* VT = (const u16*)(ws_c + OFF_VT) + (size_t)layer * S_ * NH_;
    const u16* KM = (const u16*)(ws_c + OFF_KMEM) + ((size_t)layer * NSEG_ + seg) * 8 * NH_;
    const u16* VM = (const u16*)(ws_c + OFF_VMEMT) + ((size_t)layer * NSEG_ + seg) * NH_ * 8;
    f32x16 s1, s0;
#pragma unroll
    for (int i = 0; i < 16; ++i) { s1[i] = 0.f; s0[i] = 0.f; }
    const bf16x8 zero8 = {0, 0, 0, 0, 0, 0, 0, 0};
#pragma unroll 4
    for (int ks = 0; ks < 16; ++ks) {
        bf16x8 qf = *(const bf16x8*)(Q + (size_t)(t0 + r) * NH_ + h * DH_ + ks * 16 + hh * 8);
        bf16x8 kf = *(const bf16x8*)(K + (size_t)(t0 + r) * NH_ + h * DH_ + ks * 16 + hh * 8);
        bf16x8 mf = zero8;
        if (r < 8) mf = *(const bf16x8*)(KM + (size_t)r * NH_ + h * DH_ + ks * 16 + hh * 8);
        s1 = MFMA32(kf, qf, s1);
        s0 = MFMA32(mf, qf, s0);
    }
    float sc[16], sm[4];
    float mx = -3.0e38f;
#pragma unroll
    for (int reg = 0; reg < 16; ++reg) {
        int key = (reg & 3) + 8 * (reg >> 2) + 4 * hh;
        float v = rb(s1[reg]) * 0.0625f;
        v = (key <= r) ? v : -1000000000.0f;
        sc[reg] = v; mx = fmaxf(mx, v);
    }
#pragma unroll
    for (int reg = 0; reg < 4; ++reg) { float v = rb(s0[reg]) * 0.0625f; sm[reg] = v; mx = fmaxf(mx, v); }
    mx = fmaxf(mx, __shfl_xor(mx, 32));
    float sum = 0.f;
#pragma unroll
    for (int reg = 0; reg < 16; ++reg) { sc[reg] = __expf(sc[reg] - mx); sum += sc[reg]; }
#pragma unroll
    for (int reg = 0; reg < 4; ++reg) { sm[reg] = __expf(sm[reg] - mx); sum += sm[reg]; }
    sum += __shfl_xor(sum, 32);
    const float inv = 1.0f / sum;
    bf16x8 pf[3];
#pragma unroll
    for (int s = 0; s < 2; ++s)
#pragma unroll
        for (int j = 0; j < 8; ++j) pf[s][j] = (short)f2bf(sc[8 * s + j] * inv);
#pragma unroll
    for (int j = 0; j < 4; ++j) { pf[2][j] = (short)f2bf(sm[j] * inv); pf[2][4 + j] = 0; }
    for (int et = 0; et < 8; ++et) {
        f32x16 o;
#pragma unroll
        for (int i = 0; i < 16; ++i) o[i] = 0.f;
        const u16* vrow = VT + (size_t)(h * DH_ + et * 32 + r) * S_ + t0;
#pragma unroll
        for (int s = 0; s < 2; ++s) {
            v2u a = *(const v2u*)(vrow + 16 * s + 4 * hh);
            v2u b = *(const v2u*)(vrow + 16 * s + 8 + 4 * hh);
            v4u w = {a.x, a.y, b.x, b.y};
            o = MFMA32(__builtin_bit_cast(bf16x8, w), pf[s], o);
        }
        {
            v2u a = *(const v2u*)(VM + (size_t)(h * DH_ + et * 32 + r) * 8 + 4 * hh);
            v4u w = {a.x, a.y, 0u, 0u};
            o = MFMA32(__builtin_bit_cast(bf16x8, w), pf[2], o);
        }
#pragma unroll
        for (int g4 = 0; g4 < 4; ++g4) {
            v2u v = {pack2(o[4 * g4], o[4 * g4 + 1]), pack2(o[4 * g4 + 2], o[4 * g4 + 3])};
            *(v2u*)(so + (wave * 32 + r) * OS + et * 32 + 8 * g4 + 4 * hh) = v;
        }
    }
    __syncthreads();
    const u16* WoT = (const u16*)(ws_c + OFF_WOT);
    float* part = (float*)(ws + OFF_PART) + (size_t)h * S_ * D_;
    for (int dt = wave; dt < 14; dt += 4) {
        f32x16 acc[4];
#pragma unroll
        for (int b = 0; b < 4; ++b)
#pragma unroll
            for (int i = 0; i < 16; ++i) acc[b][i] = 0.f;
        const u16* wrow = WoT + (size_t)(dt * 32 + r) * NH_ + h * DH_ + hh * 8;
#pragma unroll 4
        for (int ks = 0; ks < 16; ++ks) {
            bf16x8 wf = *(const bf16x8*)(wrow + ks * 16);
#pragma unroll
            for (int b = 0; b < 4; ++b) {
                bf16x8 of = *(const bf16x8*)(so + (b * 32 + r) * OS + ks * 16 + hh * 8);
                acc[b] = MFMA32(wf, of, acc[b]);
            }
        }
#pragma unroll
        for (int b = 0; b < 4; ++b) {
            int t = grp * 128 + b * 32 + r;
#pragma unroll
            for (int g4 = 0; g4 < 4; ++g4) {
                f32x4 v = {acc[b][4 * g4], acc[b][4 * g4 + 1], acc[b][4 * g4 + 2], acc[b][4 * g4 + 3]};
                *(f32x4*)(part + (size_t)t * D_ + dt * 32 + 8 * g4 + 4 * hh) = v;
            }
        }
    }
}

__global__ void __launch_bounds__(256) k_red(const char* __restrict__ ws_c, char* __restrict__ ws, u16* __restrict__ out, int layer) {
    __shared__ u16 yt[32][450];
    const int t0 = blockIdx.x * 32, tid = threadIdx.x, lane = tid & 63, wave = tid >> 6;
    if (layer == 0) {
        const u16* yT = (const u16*)(ws_c + OFF_YT);
        for (int i = tid; i < 448 * 16; i += 256) {
            int d = i >> 4, tp = i & 15;
            unsigned w = *(const unsigned*)(yT + (size_t)d * S_ + t0 + 2 * tp);
            yt[2 * tp][d] = (u16)(w & 0xffff); yt[2 * tp + 1][d] = (u16)(w >> 16);
        }
        __syncthreads();
    }
    const float* part = (const float*)(ws_c + OFF_PART);
    const u16* xin = (const u16*)(ws_c + (layer == 0 ? OFF_X1 : OFF_X3));
    for (int rr = wave; rr < 32; rr += 4) {
        int t = t0 + rr;
        float x[7], ln[7];
#pragma unroll
        for (int i = 0; i < 7; ++i) {
            float s = 0.f;
#pragma unroll
            for (int h = 0; h < 16; ++h) s += part[((size_t)h * S_ + t) * D_ + lane + 64 * i];
            x[i] = rb(bf2f(xin[(size_t)t * D_ + lane + 64 * i]) + rb(s));
        }
        if (layer == 1) {
#pragma unroll
            for (int i = 0; i < 7; ++i) out[(size_t)t * D_ + lane + 64 * i] = f2bf(x[i]);
        } else {
            layernorm7(x, ln);
#pragma unroll
            for (int i = 0; i < 7; ++i) x[i] = rb(x[i] + rb(ln[i] * bf2f(yt[rr][lane + 64 * i])));
            layernorm7(x, ln);
            u16* x3 = (u16*)(ws + OFF_X3); u16* hb = (u16*)(ws + OFF_HB) + (size_t)S_ * D_;
#pragma unroll
            for (int i = 0; i < 7; ++i) { x3[(size_t)t * D_ + lane + 64 * i] = f2bf(x[i]); hb[(size_t)t * D_ + lane + 64 * i] = f2bf(ln[i]); }
        }
    }
}

constexpr unsigned SENT = 0xFFFFFFFFu;
DI bool okv(v4u v) { return v.x != SENT && v.y != SENT && v.z != SENT && v.w != SENT; }
DI unsigned san(unsigned w) { return w == SENT ? 0x7FC07FC0u : w; }
DI v4u san4(v4u v) { v4u o = {san(v.x), san(v.y), san(v.z), san(v.w)}; return o; }
constexpr int SPIN_CAP = 1 << 18;
constexpr int MS = 456;
constexpr int SSR = 44;

DI float xmax16(float v) { auto r = __builtin_amdgcn_permlane16_swap(__float_as_uint(v), __float_as_uint(v), false, false); return fmaxf(__uint_as_float(r[0]), __uint_as_float(r[1])); }
DI float xmax32(float v) { auto r = __builtin_amdgcn_permlane32_swap(__float_as_uint(v), __float_as_uint(v), false, false); return fmaxf(__uint_as_float(r[0]), __uint_as_float(r[1])); }
DI float xsum16(float v) { auto r = __builtin_amdgcn_permlane16_swap(__float_as_uint(v), __float_as_uint(v), false, false); return __uint_as_float(r[0]) + __uint_as_float(r[1]); }
DI float xsum32(float v) { auto r = __builtin_amdgcn_permlane32_swap(__float_as_uint(v), __float_as_uint(v), false, false); return __uint_as_float(r[0]) + __uint_as_float(r[1]); }
template <int CTRL> DI float dpp_f(float v) { return __uint_as_float(__builtin_amdgcn_update_dpp(0u, __float_as_uint(v), CTRL, 0xF, 0xF, false)); }
DI float rowsum16(float v) {
    v += dpp_f<0x128>(v);
    v += dpp_f<0x124>(v);
    v += dpp_f<0x122>(v);
    v += dpp_f<0x121>(v);
    return v;
}

#define STAMP(i)

__global__ void __launch_bounds__(512, 2) k_chain(const u16* __restrict__ mem0, char* __restrict__ ws, int start0, int start1, int nticks) {
    __shared__ __attribute__((aligned(16))) u16 sMem[16 * MS];
    __shared__ __attribute__((aligned(16))) u16 sQ[16 * 72];
    __shared__ __attribute__((aligned(16))) u16 sKm[16 * 72];
    __shared__ __attribute__((aligned(16))) u16 sVT[256 * 24];
    __shared__ __attribute__((aligned(16))) float sS[16 * SSR];
    __shared__ __attribute__((aligned(16))) u16 sO[16 * 264];
    __shared__ __attribute__((aligned(16))) u16 sM[112];
    const int wg = blockIdx.x, tid = threadIdx.x, lane = tid & 63;
    const int wave = __builtin_amdgcn_readfirstlane(tid >> 6);
    const int h = wg >> 2, g = wg & 3;
    const int r16 = lane & 15, kg = lane >> 4;
    const u16* WT = (const u16*)(ws + OFF_WQKVT);
    const u16* WoT = (const u16*)(ws + OFF_WOT);
    auto rK = __builtin_amdgcn_make_buffer_rsrc(ws + OFF_K, 0, (unsigned)((size_t)2 * S_ * NH_ * 2), 0x00020000);
    auto rV = __builtin_amdgcn_make_buffer_rsrc(ws + OFF_VT, 0, (unsigned)((size_t)2 * S_ * NH_ * 2), 0x00020000);
    auto rKM = __builtin_amdgcn_make_buffer_rsrc(ws + OFF_KMEM, 0, (unsigned)((size_t)2 * NSEG_ * 8 * NH_ * 2), 0x00020000);
    auto rVM = __builtin_amdgcn_make_buffer_rsrc(ws + OFF_VMEMT, 0, (unsigned)((size_t)2 * NSEG_ * 8 * NH_ * 2), 0x00020000);
    unsigned* errw = (unsigned*)(ws + OFF_ERR);
    auto rA = __builtin_amdgcn_make_buffer_rsrc(ws + OFF_XA, 0, (unsigned)SZ_XA, 0x00020000);
    auto rB = __builtin_amdgcn_make_buffer_rsrc(ws + OFF_XB, 0, (unsigned)SZ_XB, 0x00020000);
    auto rC = __builtin_amdgcn_make_buffer_rsrc(ws + OFF_XC, 0, (unsigned)SZ_XC, 0x00020000);
    const v4u sent4 = {SENT, SENT, SENT, SENT};
    const bf16x8 zero8 = {0, 0, 0, 0, 0, 0, 0, 0};
    const v4u zero4 = {0u, 0u, 0u, 0u};

    bf16x8 wreg[30];
    {
        const int w4 = wave & 3;
        const int m0 = wave < 4 ? 0 : 2;
#pragma unroll
        for (int ks = 0; ks < 14; ++ks)
            wreg[ks] = *(const bf16x8*)(WT + (size_t)(m0 * 4096 + h * DH_ + g * 64 + w4 * 16 + r16) * D_ + ks * 32 + kg * 8);
        if (wave < 4) {
#pragma unroll
            for (int ks = 0; ks < 14; ++ks)
                wreg[14 + ks] = *(const bf16x8*)(WT + (size_t)(1 * 4096 + h * DH_ + g * 64 + w4 * 16 + r16) * D_ + ks * 32 + kg * 8);
            wreg[28] = zero8; wreg[29] = zero8;
        } else {
#pragma unroll
            for (int tl = 0; tl < 2; ++tl)
#pragma unroll
                for (int ks = 0; ks < 8; ++ks) {
                    int dt = 2 * w4 + tl;
                    wreg[14 + tl * 8 + ks] = (dt < 7) ? *(const bf16x8*)(WoT + (size_t)(g * 112 + dt * 16 + r16) * NH_ + h * DH_ + ks * 32 + kg * 8) : zero8;
                }
        }
    }
    for (int i = tid; i < 16 * MS; i += 512) sMem[i] = 0;
    for (int i = tid; i < 256 * 24; i += 512) sVT[i] = 0;

    bf16x8 kxf[2] = {zero8, zero8};
    bf16x8 vxf[2][2];
    auto load_kx = [&](int T) {
        if (wave >= 1 && wave <= 4) {
            const int c = (wave - 1) >> 1, hf = (wave - 1) & 1;
            int tc = T - (c ? start1 : start0);
            tc = tc < 0 ? 0 : (tc > NSEG_ - 1 ? NSEG_ - 1 : tc);
#pragma unroll
            for (int ks = 0; ks < 2; ++ks)
                kxf[ks] = __builtin_bit_cast(bf16x8, __builtin_amdgcn_raw_buffer_load_b128(rK, (unsigned)(((c * S_ + tc * SEG_ + hf * 16 + r16) * NH_ + h * DH_ + g * 64 + ks * 32 + kg * 8) * 2), 0, 0));
        }
    };
    auto load_vx = [&](int T) {
#pragma unroll
        for (int c = 0; c < 2; ++c) {
            int tc = T - (c ? start1 : start0);
            tc = tc < 0 ? 0 : (tc > NSEG_ - 1 ? NSEG_ - 1 : tc);
#pragma unroll
            for (int tl = 0; tl < 2; ++tl)
                vxf[c][tl] = __builtin_bit_cast(bf16x8, __builtin_amdgcn_raw_buffer_load_b128(rV, (unsigned)(((c * NH_ + h * DH_ + (wave * 2 + tl) * 16 + r16) * S_ + tc * SEG_ + kg * 8) * 2), 0, 0));
        }
    };
    load_kx(0); load_vx(0);
    __syncthreads();

    for (int T = 0; T < nticks; ++T) {
        const int sl = T & (RING - 1), sn = (T + 1) & (RING - 1);
        const int tc0 = T - start0, tc1 = T - start1;
        const bool act0 = start0 >= 0 && tc0 >= 0 && tc0 < NSEG_;
        const bool act1 = start1 >= 0 && tc1 >= 0 && tc1 < NSEG_;
        const unsigned cOff = (unsigned)(((sl * NCH + wg) * 896 + tid) * 16);
        if (tid < 448) {
            v4u v0 = zero4, v1 = zero4;
            if (T > 0) {
                int sp = 0;
                for (; sp < SPIN_CAP; ++sp) {
                    asm volatile("" ::: "memory");
                    v0 = __builtin_amdgcn_raw_buffer_load_b128(rC, cOff, 0, 16);
                    v1 = __builtin_amdgcn_raw_buffer_load_b128(rC, cOff + 448 * 16, 0, 16);
                    if (okv(v0) && okv(v1)) break;
                }
                if (sp == SPIN_CAP) __hip_atomic_store(errw, 1u, __ATOMIC_RELAXED, __HIP_MEMORY_SCOPE_AGENT);
            }
            const int row = tid / 56, c = tid % 56;
            if (!act0) v0 = zero4;
            if (!act1) v1 = zero4;
            if ((act0 && tc0 == 0) || (act1 && tc1 == 0)) {
                v4u m = *(const v4u*)(mem0 + (size_t)row * D_ + c * 8);
                if (act0 && tc0 == 0) v0 = m;
                if (act1 && tc1 == 0) v1 = m;
            }
            *(v4u*)(sMem + row * MS + c * 8) = v0;
            *(v4u*)(sMem + (8 + row) * MS + c * 8) = v1;
        }
        __syncthreads();
        STAMP(0)
        const int cr = r16 >> 3;
        if (wave < 4) {
            f32x4 aq = {0.f, 0.f, 0.f, 0.f}, ak = {0.f, 0.f, 0.f, 0.f};
#pragma unroll
            for (int ks = 0; ks < 14; ++ks) {
                bf16x8 mf = *(const bf16x8*)(sMem + r16 * MS + ks * 32 + kg * 8);
                aq = MFMA16(wreg[ks], mf, aq);
                ak = MFMA16(wreg[14 + ks], mf, ak);
            }
            v2u vq = {pack2(aq[0], aq[1]), pack2(aq[2], aq[3])};
            v2u vk = {pack2(ak[0], ak[1]), pack2(ak[2], ak[3])};
            *(v2u*)(sQ + r16 * 72 + wave * 16 + 4 * kg) = vq;
            *(v2u*)(sKm + r16 * 72 + wave * 16 + 4 * kg) = vk;
            if (cr ? act1 : act0) {
                const int tc = cr ? tc1 : tc0;
                __builtin_amdgcn_raw_buffer_store_b64(vk, rKM, (unsigned)((((cr * NSEG_ + tc) * 8 + (r16 & 7)) * NH_ + h * DH_ + g * 64 + wave * 16 + 4 * kg) * 2), 0, 16);
            }
        } else {
            const int w4 = wave - 4;
            f32x4 av = {0.f, 0.f, 0.f, 0.f};
#pragma unroll
            for (int ks = 0; ks < 14; ++ks) {
                bf16x8 mf = *(const bf16x8*)(sMem + r16 * MS + ks * 32 + kg * 8);
                av = MFMA16(mf, wreg[ks], av);
            }
            v2u vv = {san(pack2(av[0], av[1])), san(pack2(av[2], av[3]))};
            const int e = w4 * 16 + r16;
#pragma unroll
            for (int c = 0; c < GG; ++c)
                __builtin_amdgcn_raw_buffer_store_b64(vv, rA, (unsigned)((((sl * NCH + h * GG + c) * GG + g) * XA_UNITS + 320) * 16 + e * 32 + kg * 8), 0, 16);
            const int ck = kg >> 1;
            if (ck ? act1 : act0) {
                const int tc = ck ? tc1 : tc0;
                __builtin_amdgcn_raw_buffer_store_b64(vv, rVM, (unsigned)((((ck * NSEG_ + tc) * NH_ + h * DH_ + g * 64 + e) * 8 + 4 * (kg & 1)) * 2), 0, 16);
            }
        }
        __syncthreads();
        STAMP(1)
        if (wave < 5) {
            f32x4 s = {0.f, 0.f, 0.f, 0.f};
#pragma unroll
            for (int ks = 0; ks < 2; ++ks) {
                bf16x8 qf = *(const bf16x8*)(sQ + r16 * 72 + ks * 32 + kg * 8);
                bf16x8 kf = (((wave - 1) >> 1) ? act1 : act0) ? kxf[ks] : zero8;
                if (wave == 0) { v4u kl = *(const v4u*)(sKm + r16 * 72 + ks * 32 + kg * 8); asm volatile("" : "+v"(kl.x), "+v"(kl.y), "+v"(kl.z), "+v"(kl.w)); kf = __builtin_bit_cast(bf16x8, kl); }
                s = MFMA16(kf, qf, s);
            }
            v4u v = san4(__builtin_bit_cast(v4u, s));
            const bool valid = (wave == 0) ? ((kg >> 1) == cr) : (((wave - 1) >> 1) == cr);
            if (valid) {
#pragma unroll
                for (int c = 0; c < GG; ++c)
                    __builtin_amdgcn_raw_buffer_store_b128(v, rA, (unsigned)((((sl * NCH + h * GG + c) * GG + g) * XA_UNITS + wave * 64 + lane) * 16), 0, 16);
            }
        }
        if (T > 0 && tid < 448) {
            __builtin_amdgcn_raw_buffer_store_b128(sent4, rC, cOff, 0, 16);
            __builtin_amdgcn_raw_buffer_store_b128(sent4, rC, cOff + 448 * 16, 0, 16);
        }
        STAMP(2)
        const unsigned aOffS = (unsigned)(((sl * NCH + wg) * GG * XA_UNITS + tid) * 16);
        const bool svalid = (wave == 0) ? ((kg >> 1) == cr) : (((wave - 1) >> 1) == cr);
        const int vu = tid - 320;
        auto aOffV = [&](int k) { const int x = vu + 192 * k; return (unsigned)((((sl * NCH + wg) * GG + (x >> 7)) * XA_UNITS + 320 + (x & 127)) * 16); };
        const bool v3 = vu < 128;
        if (wave < 5) {
            if (svalid) {
                v4u v[GG];
                int sp = 0;
                for (; sp < SPIN_CAP; ++sp) {
                    asm volatile("" ::: "memory");
                    bool ok = true;
#pragma unroll
                    for (int p = 0; p < GG; ++p) v[p] = __builtin_amdgcn_raw_buffer_load_b128(rA, aOffS + p * XA_UNITS * 16, 0, 16);
#pragma unroll
                    for (int p = 0; p < GG; ++p) ok = ok && okv(v[p]);
                    if (ok) break;
                }
                if (sp == SPIN_CAP) __hip_atomic_store(errw, 2u, __ATOMIC_RELAXED, __HIP_MEMORY_SCOPE_AGENT);
                f32x4 s = __builtin_bit_cast(f32x4, v[0]);
#pragma unroll
                for (int p = 1; p < GG; ++p) s += __builtin_bit_cast(f32x4, v[p]);
                const int col = (wave == 0) ? (kg & 1) * 4 : 8 + ((wave - 1) & 1) * 16 + kg * 4;
                *(f32x4*)(sS + r16 * SSR + col) = s;
            }
        } else {
            v4u v0, v1, v2 = zero4;
            int sp = 0;
            for (; sp < SPIN_CAP; ++sp) {
                asm volatile("" ::: "memory");
                v0 = __builtin_amdgcn_raw_buffer_load_b128(rA, aOffV(0), 0, 16);
                v1 = __builtin_amdgcn_raw_buffer_load_b128(rA, aOffV(1), 0, 16);
                if (v3) v2 = __builtin_amdgcn_raw_buffer_load_b128(rA, aOffV(2), 0, 16);
                if (okv(v0) && okv(v1) && okv(v2)) break;
            }
            if (sp == SPIN_CAP) __hip_atomic_store(errw, 3u, __ATOMIC_RELAXED, __HIP_MEMORY_SCOPE_AGENT);
            { const int x = vu; *(v4u*)(sVT + (x >> 1) * 24 + (x & 1) * 8) = v0; }
            { const int x = vu + 192; *(v4u*)(sVT + (x >> 1) * 24 + (x & 1) * 8) = v1; }
            if (v3) { const int x = vu + 384; *(v4u*)(sVT + (x >> 1) * 24 + (x & 1) * 8) = v2; }
        }
        __syncthreads();
        STAMP(3)
        {
            float xs[8], ms[8];
            {
                f32x4 a = *(const f32x4*)(sS + r16 * SSR + 8 + kg * 8), b = *(const f32x4*)(sS + r16 * SSR + 12 + kg * 8);
                f32x4 c = *(const f32x4*)(sS + r16 * SSR), d = *(const f32x4*)(sS + r16 * SSR + 4);
#pragma unroll
                for (int j = 0; j < 4; ++j) { xs[j] = a[j]; xs[4 + j] = b[j]; ms[j] = c[j]; ms[4 + j] = d[j]; }
            }
            const bool hasm = kg == cr;
            float mx = -3.0e38f;
#pragma unroll
            for (int j = 0; j < 8; ++j) {
                xs[j] = rb(xs[j]) * 0.0625f; mx = fmaxf(mx, xs[j]);
                ms[j] = rb(ms[j]) * 0.0625f; mx = fmaxf(mx, ms[j]);
            }
            mx = xmax16(mx); mx = xmax32(mx);
            float sum = 0.f, summ = 0.f;
#pragma unroll
            for (int j = 0; j < 8; ++j) { xs[j] = __expf(xs[j] - mx); sum += xs[j]; ms[j] = __expf(ms[j] - mx); summ += ms[j]; }
            sum = xsum16(sum); sum = xsum32(sum);
            const float inv = 1.0f / (sum + summ);
            v4u pxu, pmu;
            pxu.x = pack2(xs[0] * inv, xs[1] * inv); pxu.y = pack2(xs[2] * inv, xs[3] * inv); pxu.z = pack2(xs[4] * inv, xs[5] * inv); pxu.w = pack2(xs[6] * inv, xs[7] * inv);
            pmu.x = pack2(ms[0] * inv, ms[1] * inv); pmu.y = pack2(ms[2] * inv, ms[3] * inv); pmu.z = pack2(ms[4] * inv, ms[5] * inv); pmu.w = pack2(ms[6] * inv, ms[7] * inv);
            const bf16x8 px = __builtin_bit_cast(bf16x8, pxu);
            const bf16x8 pm = hasm ? __builtin_bit_cast(bf16x8, pmu) : zero8;
            const bf16x8 px0 = cr == 0 ? px : zero8, px1 = cr == 1 ? px : zero8;
#pragma unroll
            for (int tl = 0; tl < 2; ++tl) {
                const int et = wave * 2 + tl;
                bf16x8 vm = zero8;
                if (kg < 2) vm = *(const bf16x8*)(sVT + (et * 16 + r16) * 24 + kg * 8);
                f32x4 o = {0.f, 0.f, 0.f, 0.f};
                o = MFMA16(vm, pm, o);
                o = MFMA16(act0 ? vxf[0][tl] : zero8, px0, o);
                o = MFMA16(act1 ? vxf[1][tl] : zero8, px1, o);
                v2u v = {pack2(o[0], o[1]), pack2(o[2], o[3])};
                *(v2u*)(sO + r16 * 264 + et * 16 + 4 * kg) = v;
            }
        }
        load_kx(T + 1); load_vx(T + 1);
        __syncthreads();
        STAMP(4)
        if (T + 1 < nticks) {
            if (wave >= 4) {
                const int w4 = wave - 4;
#pragma unroll
                for (int tl = 0; tl < 2; ++tl) {
                    const int dt = 2 * w4 + tl;
                    if (dt < 7) {
                        f32x4 a = {0.f, 0.f, 0.f, 0.f};
#pragma unroll
                        for (int ks = 0; ks < 8; ++ks) {
                            bf16x8 of = *(const bf16x8*)(sO + r16 * 264 + ks * 32 + kg * 8);
                            a = MFMA16(wreg[14 + tl * 8 + ks], of, a);
                        }
                        v4u v = san4(__builtin_bit_cast(v4u, a));
                        __builtin_amdgcn_raw_buffer_store_b128(v, rB, (unsigned)((((sl * NCH + r16 * GG + g) * 16 + h) * 32 + dt * 4 + kg) * 16), 0, 16);
                    }
                }
            }
        }
        if (wave < 5) {
            if (svalid) {
#pragma unroll
                for (int p = 0; p < GG; ++p) __builtin_amdgcn_raw_buffer_store_b128(sent4, rA, aOffS + p * XA_UNITS * 16, 0, 16);
            }
        } else {
            __builtin_amdgcn_raw_buffer_store_b128(sent4, rA, aOffV(0), 0, 16);
            __builtin_amdgcn_raw_buffer_store_b128(sent4, rA, aOffV(1), 0, 16);
            if (v3) __builtin_amdgcn_raw_buffer_store_b128(sent4, rA, aOffV(2), 0, 16);
        }
        STAMP(5)
        if (T + 1 < nticks) {
            if (wave < 7) {
                const int p = r16, u = wave * 4 + kg;
                const unsigned bOff = (unsigned)((((sl * NCH + wg) * 16 + p) * 32 + u) * 16);
                v4u v;
                int sp = 0;
                for (; sp < SPIN_CAP; ++sp) {
                    asm volatile("" ::: "memory");
                    v = __builtin_amdgcn_raw_buffer_load_b128(rB, bOff, 0, 16);
                    if (okv(v)) break;
                }
                if (sp == SPIN_CAP) __hip_atomic_store(errw, 4u, __ATOMIC_RELAXED, __HIP_MEMORY_SCOPE_AGENT);
                float s0 = rowsum16(__uint_as_float(v.x)), s1 = rowsum16(__uint_as_float(v.y)), s2 = rowsum16(__uint_as_float(v.z)), s3 = rowsum16(__uint_as_float(v.w));
                v2u o = {san(pack2(s0, s1)), san(pack2(s2, s3))};
                if (p == 0) *(v2u*)(sM + u * 4) = o;
                __builtin_amdgcn_raw_buffer_store_b128(sent4, rB, bOff, 0, 16);
            }
            __syncthreads();
            STAMP(6)
            for (int i = tid; i < NCH * 14; i += 512) {
                int c = i / 14, k = i % 14;
                v4u v = *(const v4u*)(sM + k * 8);
                __builtin_amdgcn_raw_buffer_store_b128(v, rC, (unsigned)(((sn * NCH + c) * 896 + h * 56 + g * 14 + k) * 16), 0, 16);
            }
        }
        STAMP(7)
    }
}

extern "C" void kernel_launch(void* const* d_in, const int* in_sizes, int n_in, void* d_out, int out_size, void* d_ws, size_t ws_size, hipStream_t stream) {
    const u16* A_q = (const u16*)d_in[0];
    const u16* A_k = (const u16*)d_in[1];
    const u16* A_v = (const u16*)d_in[2];
    const u16* A_shk = (const u16*)d_in[3];
    const u16* A_ssmk = (const u16*)d_in[4];
    const u16* A_ssmD = (const u16*)d_in[5];
    const u16* B_mem0 = (const u16*)d_in[7];
    const u16* B_Wq = (const u16*)d_in[8];
    const u16* B_Wk = (const u16*)d_in[9];
    const u16* B_Wv = (const u16*)d_in[10];
    const u16* B_Wo = (const u16*)d_in[11];
    char* ws = (char*)d_ws;
    u16* out = (u16*)d_out;

    k_prep<<<3 * 448 + 448 + 224, 256, 0, stream>>>(B_Wq, B_Wk, B_Wv, B_Wo, A_k, A_v, A_shk, ws);
    k_conv<<<448, 256, 0, stream>>>(A_ssmk, A_ssmD, ws);
    k_stage1<<<64, 256, 0, stream>>>(A_q, ws);
    for (int layer = 0; layer < 2; ++layer) {
        k_qkv<<<96 * 16, 256, 0, stream>>>(ws, ws, layer);
        hipMemsetAsync(ws + OFF_XA, 0xFF, OFF_ERR - OFF_XA, stream);
        hipMemsetAsync(ws + OFF_ERR, 0, 256, stream);
        k_chain<<<NCH, 512, 0, stream>>>(B_mem0, ws, layer == 0 ? 0 : -1, layer == 0 ? -1 : 0, 64);
        k_att_out<<<16 * 16, 256, 0, stream>>>(ws, ws, layer);
        k_red<<<64, 256, 0, stream>>>(ws, ws, out, layer);
    }
}
```

```cpp
#include <hip/hip_runtime.h>
#include <hip/hip_bf16.h>
#include <stdint.h>

constexpr int S_ = 2048, D_ = 448, H_ = 16, DH_ = 256, NH_ = 4096, SEG_ = 32, NSEG_ = 64, MM_ = 8;

typedef unsigned short u16;
typedef __attribute__((ext_vector_type(8))) short bf16x8;
typedef __attribute__((ext_vector_type(4))) float f32x4;
typedef __attribute__((ext_vector_type(16))) float f32x16;
typedef unsigned v4u __attribute__((ext_vector_type(4)));
typedef unsigned v2u __attribute__((ext_vector_type(2)));

#define DI __device__ __forceinline__
#define MFMA32(a, b, c) __builtin_amdgcn_mfma_f32_32x32x16_bf16((a), (b), (c), 0, 0, 0)
#define MFMA16(a, b, c) __builtin_amdgcn_mfma_f32_16x16x32_bf16((a), (b), (c), 0, 0, 0)

DI float bf2f(u16 v) { return __uint_as_float(((unsigned)v) << 16); }
typedef __attribute__((ext_vector_type(2))) __bf16 bf16x2_t;
typedef __attribute__((ext_vector_type(2))) float f32x2;
DI unsigned pack2(float a, float b) { f32x2 f = {a, b}; return __builtin_bit_cast(unsigned, __builtin_convertvector(f, bf16x2_t)); }
DI u16 f2bf(float f) { return (u16)(pack2(f, 0.f) & 0xffffu); }
DI float rb(float f) { return __uint_as_float(pack2(f, 0.f) << 16); }

constexpr size_t al(size_t x) { return (x + 255) & ~(size_t)255; }
constexpr size_t OFF_WQKVT = 0;
constexpr size_t OFF_WOT   = OFF_WQKVT + al((size_t)12288 * 448 * 2);
constexpr size_t OFF_WOF   = OFF_WOT + al((size_t)448 * 4096 * 2);
constexpr size_t OFF_KVT   = OFF_WOF + al((size_t)448 * 4096 * 2);
constexpr size_t OFF_YT    = OFF_KVT + al((size_t)448 * 2048 * 2);
constexpr size_t OFF_Y     = OFF_YT + al((size_t)448 * 2048 * 2);
constexpr size_t OFF_X1    = OFF_Y + al((size_t)448 * 2048 * 2);
constexpr size_t OFF_X3    = OFF_X1 + al((size_t)2048 * 448 * 2);
constexpr size_t OFF_HB    = OFF_X3 + al((size_t)2048 * 448 * 2);
constexpr size_t OFF_Q     = OFF_HB + al((size_t)2 * 2048 * 448 * 2);
constexpr size_t OFF_K     = OFF_Q + al((size_t)2 * 2048 * 4096 * 2);
constexpr size_t OFF_VT    = OFF_K + al((size_t)2 * 2048 * 4096 * 2);
constexpr size_t OFF_KMEM  = OFF_VT + al((size_t)2 * 2048 * 4096 * 2);
constexpr size_t OFF_VMEMT = OFF_KMEM + al((size_t)2 * 64 * 8 * 4096 * 2);
constexpr size_t OFF_PART  = OFF_VMEMT + al((size_t)2 * 64 * 8 * 4096 * 2);
constexpr int RING = 4, NCH = 64, GG = 4;
constexpr int XA_UNITS = 320 + 128;
constexpr size_t OFF_XA    = OFF_PART + al((size_t)16 * 2048 * 448 * 4);
constexpr size_t SZ_XA     = (size_t)RING * NCH * GG * XA_UNITS * 16;
constexpr size_t OFF_XB    = OFF_XA + al(SZ_XA);
constexpr size_t SZ_XB     = (size_t)RING * NCH * 16 * 32 * 16;
constexpr size_t OFF_XC    = OFF_XB + al(SZ_XB);
constexpr size_t SZ_XC     = (size_t)RING * NCH * 896 * 16;
constexpr size_t OFF_FL    = OFF_XC + al(SZ_XC);
constexpr int FL_TICKET = 0, FL_ERR = 1, FL_QKV = 16, FL_MEM = FL_QKV + 512, FL_ATT = FL_MEM + 512, FL_RED = FL_ATT + 32, FL_N = FL_RED + 32;
constexpr size_t SZ_FL     = (size_t)FL_N * 64;
constexpr size_t WS_END    = OFF_FL + al(SZ_FL);

__device__ void transpose_tile(const u16* __restrict__ src, int C, int r0, int c0, u16* __restrict__ dst, int ldd, int dr0, u16 (*tile)[66]) {
    const int tid = threadIdx.x;
    for (int i = tid; i < 64 * 8; i += 256) {
        int r = i >> 3, cq = i & 7;
        v4u v = *(const v4u*)(src + (size_t)(r0 + r) * C + c0 + cq * 8);
        unsigned w[4] = {v.x, v.y, v.z, v.w};
#pragma unroll
        for (int j = 0; j < 4; ++j) { tile[r][cq * 8 + 2 * j] = (u16)(w[j] & 0xffff); tile[r][cq * 8 + 2 * j + 1] = (u16)(w[j] >> 16); }
    }
    __syncthreads();
    for (int i = tid; i < 64 * 8; i += 256) {
        int c = i >> 3, rq = i & 7;
        unsigned w[4];
#pragma unroll
        for (int j = 0; j < 4; ++j) w[j] = (unsigned)tile[rq * 8 + 2 * j][c] | ((unsigned)tile[rq * 8 + 2 * j + 1][c] << 16);
        v4u v = {w[0], w[1], w[2], w[3]};
        *(v4u*)(dst + (size_t)(dr0 + c0 + c) * ldd + r0 + rq * 8) = v;
    }
    __syncthreads();
}

__global__ void __launch_bounds__(256) k_prep(const u16* __restrict__ Wq, const u16* __restrict__ Wk, const u16* __restrict__ Wv, const u16* __restrict__ Wo,
                                               const u16* __restrict__ kin, const u16* __restrict__ vin, const u16* __restrict__ shk, char* __restrict__ ws) {
    __shared__ u16 tile[64][66];
    int b = blockIdx.x;
    if (b < 3 * 448) {
        int w = b / 448, t = b % 448;
        const u16* src = w == 0 ? Wq : (w == 1 ? Wk : Wv);
        int r0 = (t / 64) * 64, c0 = (t % 64) * 64;
        transpose_tile(src, 4096, r0, c0, (u16*)(ws + OFF_WQKVT), 448, w * 4096, tile);
        return;
    }
    b -= 3 * 448;
    if (b < 448) {
        int r0 = (b / 7) * 64, c0 = (b % 7) * 64;
        transpose_tile(Wo, 448, r0, c0, (u16*)(ws + OFF_WOT), 4096, 0, tile);
        u16* wof = (u16*)(ws + OFF_WOF);
        for (int i = threadIdx.x; i < 64 * 8; i += 256) {
            int dl = i & 63, eq = i >> 6;
            int e0 = r0 + eq * 8, d = c0 + dl;
            int h = e0 >> 8, ee = e0 & 255, ks = ee >> 4, hh = (ee >> 3) & 1, dt = d >> 5, r = d & 31;
            unsigned w[4];
#pragma unroll
            for (int j = 0; j < 4; ++j) w[j] = (unsigned)tile[eq * 8 + 2 * j][dl] | ((unsigned)tile[eq * 8 + 2 * j + 1][dl] << 16);
            v4u v = {w[0], w[1], w[2], w[3]};
            *(v4u*)(wof + ((((size_t)h * 14 + dt) * 16 + ks) * 64 + hh * 32 + r) * 8) = v;
        }
        return;
    }
    b -= 448;
    {
        int t0 = (b / 7) * 64, d0 = (b % 7) * 64;
        const int tid = threadIdx.x;
        for (int i = tid; i < 64 * 64; i += 256) {
            int tt = i >> 6, dd = i & 63;
            int t = t0 + tt, d = d0 + dd;
            float acc = 0.f;
#pragma unroll
            for (int j = 0; j < 4; ++j) {
                int ts = t + j - 3;
                float kv_ = ts >= 0 ? bf2f(kin[(size_t)ts * D_ + d]) : 0.f;
                float pr = rb(kv_ * bf2f(shk[d * 4 + (3 - j)]));
                acc = rb(acc + pr);
            }
            float kvv = rb(acc * bf2f(vin[(size_t)t * D_ + d]));
            tile[dd][tt] = f2bf(kvv);
        }
        __syncthreads();
        u16* kvT = (u16*)(ws + OFF_KVT);
        for (int i = tid; i < 64 * 32; i += 256) {
            int dd = i >> 5, tp = i & 31;
            unsigned w = (unsigned)tile[dd][2 * tp] | ((unsigned)tile[dd][2 * tp + 1] << 16);
            *(unsigned*)(kvT + (size_t)(d0 + dd) * S_ + t0 + 2 * tp) = w;
        }
    }
}

constexpr int CV_GS = 2088;
__global__ void __launch_bounds__(256) k_conv(const u16* __restrict__ ssmk, const u16* __restrict__ ssmD, char* __restrict__ ws) {
    __shared__ __attribute__((aligned(16))) u16 gcp[8 * CV_GS];
    __shared__ __attribute__((aligned(16))) u16 kvs[96 * 40];
    __shared__ float ysum[2048];
    const int d = blockIdx.x, tid = threadIdx.x, lane = tid & 63, wave = tid >> 6;
    const u16* hrow = ssmk + (size_t)d * S_;
    const u16* kvT = (const u16*)(ws + OFF_KVT) + (size_t)d * S_;
    for (int i = tid; i < 8 * CV_GS; i += 256) {
        int p = i / CV_GS, x = i % CV_GS;
        int m = x + p;
        u16 v = 0;
        if (m <= 2047) v = hrow[2047 - m];
        gcp[i] = v;
    }
    for (int i = tid; i < 96 * 40; i += 256) {
        int blk = i / 40, j = i % 40;
        u16 v = 0;
        if (blk >= 32 && j < 32) v = kvT[(blk - 32) * 32 + j];
        kvs[i] = v;
    }
    for (int i = tid; i < 2048; i += 256) ysum[i] = 0.f;
    __syncthreads();
    f32x16 acc0, acc1;
#pragma unroll
    for (int i = 0; i < 16; ++i) { acc0[i] = 0.f; acc1[i] = 0.f; }
    const int r = lane & 31, hh = lane >> 5;
    for (int dl = wave; dl < 64; dl += 4) {
#pragma unroll
        for (int ks = 0; ks < 2; ++ks) {
            int a = 32 * dl + r - 16 * ks - 8 * hh;
            int base = 2047 - a;
            int p = base & 7, q = base >> 3;
            bf16x8 af = *(const bf16x8*)(gcp + p * CV_GS + q * 8);
            {
                int blk = 32 + (32 + r - dl);
                bf16x8 bfrag = *(const bf16x8*)(kvs + blk * 40 + 16 * ks + 8 * hh);
                acc1 = MFMA32(af, bfrag, acc1);
            }
            if (dl < 32) {
                int blk = 32 + (r - dl);
                bf16x8 bfrag = *(const bf16x8*)(kvs + blk * 40 + 16 * ks + 8 * hh);
                acc0 = MFMA32(af, bfrag, acc0);
            }
        }
    }
#pragma unroll
    for (int reg = 0; reg < 16; ++reg) {
        int j = (reg & 3) + 8 * (reg >> 2) + 4 * hh;
        atomicAdd(&ysum[32 * r + j], acc0[reg]);
        atomicAdd(&ysum[32 * (32 + r) + j], acc1[reg]);
    }
    __syncthreads();
    const float Dd = bf2f(ssmD[d]);
    u16* yT = (u16*)(ws + OFF_YT) + (size_t)d * S_;
    for (int t = tid; t < 2048; t += 256) {
        float kvv = bf2f(kvs[(32 + (t >> 5)) * 40 + (t & 31)]);
        float y = rb(rb(ysum[t]) + rb(kvv * Dd));
        yT[t] = f2bf(y);
    }
}

DI float wave_sum(float v) {
#pragma unroll
    for (int m = 32; m >= 1; m >>= 1) v += __shfl_xor(v, m);
    return v;
}
DI void layernorm7(const float (&x)[7], float (&o)[7]) {
    float s = 0.f;
#pragma unroll
    for (int i = 0; i < 7; ++i) s += x[i];
    s = wave_sum(s);
    const float mean = s * (1.0f / 448.0f);
    float q = 0.f;
#pragma unroll
    for (int i = 0; i < 7; ++i) { float dlt = x[i] - mean; q += dlt * dlt; }
    q = wave_sum(q);
    const float m = rb(mean), v = rb(q * (1.0f / 448.0f));
    const float den = rb(sqrtf(rb(v + 1e-5f)));
#pragma unroll
    for (int i = 0; i < 7; ++i) o[i] = rb(rb(x[i] - m) / den);
}

__global__ void __launch_bounds__(256) k_stage1(const u16* __restrict__ x0, char* __restrict__ ws) {
    __shared__ u16 yt[32][450];
    const int t0 = blockIdx.x * 32, tid = threadIdx.x, lane = tid & 63, wave = tid >> 6;
    const u16* yT = (const u16*)(ws + OFF_YT);
    for (int i = tid; i < 448 * 16; i += 256) {
        int d = i >> 4, tp = i & 15;
        unsigned w = *(const unsigned*)(yT + (size_t)d * S_ + t0 + 2 * tp);
        yt[2 * tp][d] = (u16)(w & 0xffff); yt[2 * tp + 1][d] = (u16)(w >> 16);
    }
    __syncthreads();
    u16* x1 = (u16*)(ws + OFF_X1); u16* hb = (u16*)(ws + OFF_HB); u16* yr = (u16*)(ws + OFF_Y);
    for (int rr = wave; rr < 32; rr += 4) {
        int t = t0 + rr;
        float x[7], ln[7];
#pragma unroll
        for (int i = 0; i < 7; ++i) x[i] = bf2f(x0[(size_t)t * D_ + lane + 64 * i]);
        layernorm7(x, ln);
#pragma unroll
        for (int i = 0; i < 7; ++i) x[i] = rb(x[i] + rb(ln[i] * bf2f(yt[rr][lane + 64 * i])));
        layernorm7(x, ln);
#pragma unroll
        for (int i = 0; i < 7; ++i) {
            x1[(size_t)t * D_ + lane + 64 * i] = f2bf(x[i]); hb[(size_t)t * D_ + lane + 64 * i] = f2bf(ln[i]);
            yr[(size_t)t * D_ + lane + 64 * i] = yt[rr][lane + 64 * i];
        }
    }
}

constexpr unsigned SENT = 0xFFFFFFFFu;
DI bool okv(v4u v) { return v.x != SENT && v.y != SENT && v.z != SENT && v.w != SENT; }
DI unsigned san(unsigned w) { return w == SENT ? 0x7FC07FC0u : w; }
DI v4u san4(v4u v) { v4u o = {san(v.x), san(v.y), san(v.z), san(v.w)}; return o; }
constexpr int SPIN_CAP = 1 << 16;
constexpr int WAIT_CAP = 1 << 16;
constexpr int MS = 456;
constexpr int SSR = 44;
constexpr int GS = 72;
constexpr int OS = 264;
constexpr int LDS_BYTES = 80 * 1024;

DI float xmax16(float v) { auto r = __builtin_amdgcn_permlane16_swap(__float_as_uint(v), __float_as_uint(v), false, false); return fmaxf(__uint_as_float(r[0]), __uint_as_float(r[1])); }
DI float xmax32(float v) { auto r = __builtin_amdgcn_permlane32_swap(__float_as_uint(v), __float_as_uint(v), false, false); return fmaxf(__uint_as_float(r[0]), __uint_as_float(r[1])); }
DI float xsum16(float v) { auto r = __builtin_amdgcn_permlane16_swap(__float_as_uint(v), __float_as_uint(v), false, false); return __uint_as_float(r[0]) + __uint_as_float(r[1]); }
DI float xsum32(float v) { auto r = __builtin_amdgcn_permlane32_swap(__float_as_uint(v), __float_as_uint(v), false, false); return __uint_as_float(r[0]) + __uint_as_float(r[1]); }
template <int CTRL> DI float dpp_f(float v) { return __uint_as_float(__builtin_amdgcn_update_dpp(0u, __float_as_uint(v), CTRL, 0xF, 0xF, false)); }
DI float rowsum16(float v) {
    v += dpp_f<0x128>(v);
    v += dpp_f<0x124>(v);
    v += dpp_f<0x122>(v);
    v += dpp_f<0x121>(v);
    return v;
}

#define TR(i)
#define STAMP(i)

DI unsigned* flag_ptr(char* ws, int idx) { return (unsigned*)(ws + OFF_FL + (size_t)idx * 64); }
DI unsigned flag_load(char* ws, int idx) { return __hip_atomic_load(flag_ptr(ws, idx), __ATOMIC_RELAXED, __HIP_MEMORY_SCOPE_AGENT); }
DI void flag_add(char* ws, int idx) { __hip_atomic_fetch_add(flag_ptr(ws, idx), 1u, __ATOMIC_RELAXED, __HIP_MEMORY_SCOPE_AGENT); }
DI void wait_flag(char* ws, int idx, unsigned target) {
    if (threadIdx.x == 0) {
        int sp = 0;
        for (; sp < WAIT_CAP; ++sp) {
            if (flag_load(ws, idx) >= target) break;
            if (flag_load(ws, FL_ERR) != 0u) break;
            __builtin_amdgcn_s_sleep(8);
        }
        if (sp == WAIT_CAP) __hip_atomic_store(flag_ptr(ws, FL_ERR), 5u, __ATOMIC_RELAXED, __HIP_MEMORY_SCOPE_AGENT);
    }
    __syncthreads();
}
DI void signal_flag(char* ws, int idx) {
    __builtin_amdgcn_s_waitcnt(0x0F70);
    __syncthreads();
    if (threadIdx.x == 0) flag_add(ws, idx);
}

DI void task_qkv(char* ws, char* smem, int layer, int G, int nb, int tid) {
    u16* sW = (u16*)smem;
    u16* sH = sW + 2 * 128 * GS;
    const int lane = tid & 63, wave = tid >> 6;
    const int n0 = nb * 128, t0 = G * 128;
    const u16* WT = (const u16*)(ws + OFF_WQKVT) + (size_t)n0 * D_;
    auto rH = __builtin_amdgcn_make_buffer_rsrc(ws + OFF_HB + ((size_t)layer * S_ + t0) * D_ * 2, 0, 128 * D_ * 2, 0x00020000);
    const bool isV = nb >= 64;
    const int wr = wave >> 1, wc = wave & 1;
    const int r = lane & 31, hh = lane >> 5;
    f32x16 acc[2];
#pragma unroll
    for (int b = 0; b < 2; ++b)
#pragma unroll
        for (int i = 0; i < 16; ++i) acc[b][i] = 0.f;
    v4u pw[7][2], ph[7][2];
#pragma unroll
    for (int kb = 0; kb < 7; ++kb)
#pragma unroll
        for (int i = 0; i < 2; ++i) {
            int c = tid + 512 * i, row = c >> 3, kq = c & 7;
            ph[kb][i] = __builtin_amdgcn_raw_buffer_load_b128(rH, (row * D_ + kb * 64 + kq * 8) * 2, 0, 16);
            pw[kb][i] = *(const v4u*)(WT + (size_t)row * D_ + kb * 64 + kq * 8);
        }
#pragma unroll
    for (int i = 0; i < 2; ++i) { int c = tid + 512 * i, row = c >> 3, kq = c & 7; *(v4u*)(sW + row * GS + kq * 8) = pw[0][i]; *(v4u*)(sH + row * GS + kq * 8) = ph[0][i]; }
    __syncthreads();
#pragma unroll
    for (int kb = 0; kb < 7; ++kb) {
        const int cur = (kb & 1) * 128 * GS, nxt = ((kb + 1) & 1) * 128 * GS;
        if (kb < 6) {
#pragma unroll
            for (int i = 0; i < 2; ++i) { int c = tid + 512 * i, row = c >> 3, kq = c & 7; *(v4u*)(sW + nxt + row * GS + kq * 8) = pw[kb + 1][i]; *(v4u*)(sH + nxt + row * GS + kq * 8) = ph[kb + 1][i]; }
        }
#pragma unroll
        for (int ks = 0; ks < 4; ++ks) {
            bf16x8 fw = *(const bf16x8*)(sW + cur + (wr * 32 + r) * GS + ks * 16 + hh * 8);
            bf16x8 fh[2];
#pragma unroll
            for (int b = 0; b < 2; ++b) fh[b] = *(const bf16x8*)(sH + cur + (wc * 64 + b * 32 + r) * GS + ks * 16 + hh * 8);
            if (!isV) {
#pragma unroll
                for (int b = 0; b < 2; ++b) acc[b] = MFMA32(fw, fh[b], acc[b]);
            } else {
#pragma unroll
                for (int b = 0; b < 2; ++b) acc[b] = MFMA32(fh[b], fw, acc[b]);
            }
        }
        __syncthreads();
    }
    u16* sT = (u16*)smem;
    if (!isV) {
#pragma unroll
        for (int b = 0; b < 2; ++b) {
            int t = wc * 64 + b * 32 + r;
#pragma unroll
            for (int g4 = 0; g4 < 4; ++g4) {
                int n = wr * 32 + 8 * g4 + 4 * hh;
                v2u v = {pack2(acc[b][4 * g4], acc[b][4 * g4 + 1]), pack2(acc[b][4 * g4 + 2], acc[b][4 * g4 + 3])};
                *(v2u*)(sT + t * 136 + n) = v;
            }
        }
        __syncthreads();
        auto rD = __builtin_amdgcn_make_buffer_rsrc(ws + (nb < 32 ? OFF_Q : OFF_K) + ((size_t)layer * S_ + t0) * NH_ * 2, 0, 128 * NH_ * 2, 0x00020000);
        const int hd = (nb >> 1) & 15, c0 = (nb & 1) * 16;
#pragma unroll
        for (int k = 0; k < 4; ++k) {
            int i = tid + 512 * k, sg = i >> 9, c = (i >> 5) & 15, row = i & 31;
            v4u v = *(const v4u*)(sT + (sg * 32 + row) * 136 + c * 8);
            __builtin_amdgcn_raw_buffer_store_b128(v, rD, (((sg * 16 + hd) * 32 + c0 + c) * 32 + row) * 16, 0, 16);
        }
    } else {
#pragma unroll
        for (int b = 0; b < 2; ++b) {
            int n = wr * 32 + r;
#pragma unroll
            for (int g4 = 0; g4 < 4; ++g4) {
                int t = wc * 64 + b * 32 + 8 * g4 + 4 * hh;
                v2u v = {pack2(acc[b][4 * g4], acc[b][4 * g4 + 1]), pack2(acc[b][4 * g4 + 2], acc[b][4 * g4 + 3])};
                *(v2u*)(sT + n * 136 + t) = v;
            }
        }
        __syncthreads();
        auto rD = __builtin_amdgcn_make_buffer_rsrc(ws + OFF_VT + ((size_t)layer * S_ + t0) * NH_ * 2, 0, 128 * NH_ * 2, 0x00020000);
        const int hd = (nb >> 1) & 15, e0 = (nb & 1) * 128;
#pragma unroll
        for (int k = 0; k < 4; ++k) {
            int i = tid + 512 * k, sg = i >> 9, kc = (i >> 7) & 3, e = i & 127;
            v4u v = *(const v4u*)(sT + e * 136 + sg * 32 + kc * 8);
            __builtin_amdgcn_raw_buffer_store_b128(v, rD, (((sg * 16 + hd) * 4 + kc) * 256 + e0 + e) * 16, 0, 16);
        }
    }
}

DI void task_att(char* ws, char* smem, int layer, int G, int h, int tid, unsigned long long* trc) {
    u16* so = (u16*)smem;
    v4u* sP = (v4u*)(smem + 128 * OS * 2);
    const int lane = tid & 63, wave = tid >> 6;
    const int sg = wave & 3, role = wave >> 2;
    const int seg = G * 4 + sg;
    const int r = lane & 31, hh = lane >> 5;
    const int pr = (r & 19) | ((r & 4) << 1) | ((r & 8) >> 1);
    const size_t sh = ((size_t)layer * NSEG_ + seg) * 16 + h;
    const u16* WoF = (const u16*)(ws + OFF_WOF) + (size_t)h * 14 * 8192;
    const v4u zero4 = {0u, 0u, 0u, 0u};
    bf16x8 wf[16];
    v4u va[8][2], vm[8];
    if (role == 0) {
        auto rQ = __builtin_amdgcn_make_buffer_rsrc(ws + OFF_Q + sh * 8192 * 2, 0, 8192 * 2, 0x00020000);
        auto rK = __builtin_amdgcn_make_buffer_rsrc(ws + OFF_K + sh * 8192 * 2, 0, 8192 * 2, 0x00020000);
        auto rKM = __builtin_amdgcn_make_buffer_rsrc(ws + OFF_KMEM + sh * 2048 * 2, 0, 2048 * 2, 0x00020000);
        f32x16 s1, s0;
#pragma unroll
        for (int i = 0; i < 16; ++i) { s1[i] = 0.f; s0[i] = 0.f; }
        v4u qf[16], kf[16], mf[16];
#pragma unroll
        for (int ks = 0; ks < 16; ++ks) {
            qf[ks] = __builtin_amdgcn_raw_buffer_load_b128(rQ, ((2 * ks + hh) * 32 + r) * 16, 0, 16);
            kf[ks] = __builtin_amdgcn_raw_buffer_load_b128(rK, ((2 * ks + hh) * 32 + pr) * 16, 0, 16);
            mf[ks] = __builtin_amdgcn_raw_buffer_load_b128(rKM, ((2 * ks + hh) * 8 + (pr & 7)) * 16, 0, 16);
        }
#pragma unroll
        for (int ks = 0; ks < 16; ++ks) {
            s1 = MFMA32(__builtin_bit_cast(bf16x8, kf[ks]), __builtin_bit_cast(bf16x8, qf[ks]), s1);
            v4u m = pr < 8 ? mf[ks] : zero4;
            s0 = MFMA32(__builtin_bit_cast(bf16x8, m), __builtin_bit_cast(bf16x8, qf[ks]), s0);
        }
        TR(3)
        float sc[16], sm[8];
        float mx = -3.0e38f;
#pragma unroll
        for (int reg = 0; reg < 16; ++reg) {
            int key = 16 * (reg >> 3) + 8 * hh + (reg & 7);
            float v = rb(s1[reg]) * 0.0625f;
            v = (key <= r) ? v : -1000000000.0f;
            sc[reg] = v; mx = fmaxf(mx, v);
        }
#pragma unroll
        for (int reg = 0; reg < 8; ++reg) { float v = rb(s0[reg]) * 0.0625f; sm[reg] = v; if (hh == 0) mx = fmaxf(mx, v); }
        mx = xmax32(mx);
        float sum = 0.f;
#pragma unroll
        for (int reg = 0; reg < 16; ++reg) { sc[reg] = __expf(sc[reg] - mx); sum += sc[reg]; }
#pragma unroll
        for (int reg = 0; reg < 8; ++reg) { sm[reg] = hh == 0 ? __expf(sm[reg] - mx) : 0.f; sum += sm[reg]; }
        sum = xsum32(sum);
        const float inv = 1.0f / sum;
#pragma unroll
        for (int s = 0; s < 2; ++s) {
            v4u w = {pack2(sc[8 * s] * inv, sc[8 * s + 1] * inv), pack2(sc[8 * s + 2] * inv, sc[8 * s + 3] * inv), pack2(sc[8 * s + 4] * inv, sc[8 * s + 5] * inv), pack2(sc[8 * s + 6] * inv, sc[8 * s + 7] * inv)};
            sP[(sg * 3 + s) * 64 + lane] = w;
        }
        { v4u w = {pack2(sm[0] * inv, sm[1] * inv), pack2(sm[2] * inv, sm[3] * inv), pack2(sm[4] * inv, sm[5] * inv), pack2(sm[6] * inv, sm[7] * inv)}; sP[(sg * 3 + 2) * 64 + lane] = w; }
        TR(4)
    } else {
        auto rV = __builtin_amdgcn_make_buffer_rsrc(ws + OFF_VT + sh * 8192 * 2, 0, 8192 * 2, 0x00020000);
        auto rVM = __builtin_amdgcn_make_buffer_rsrc(ws + OFF_VMEMT + sh * 2048 * 2, 0, 2048 * 2, 0x00020000);
#pragma unroll
        for (int et = 0; et < 8; ++et) {
#pragma unroll
            for (int s = 0; s < 2; ++s) va[et][s] = __builtin_amdgcn_raw_buffer_load_b128(rV, ((2 * s + hh) * 256 + et * 32 + r) * 16, 0, 16);
            vm[et] = __builtin_amdgcn_raw_buffer_load_b128(rVM, (et * 32 + r) * 16, 0, 16);
        }
    }
#pragma unroll
    for (int ks = 0; ks < 16; ++ks) wf[ks] = *(const bf16x8*)(WoF + ((size_t)(wave * 16 + ks) * 64 + lane) * 8);
    __syncthreads();
    if (role == 1) {
        bf16x8 pf[3];
#pragma unroll
        for (int s = 0; s < 3; ++s) pf[s] = __builtin_bit_cast(bf16x8, sP[(sg * 3 + s) * 64 + lane]);
#pragma unroll
        for (int et = 0; et < 8; ++et) {
            f32x16 o;
#pragma unroll
            for (int i = 0; i < 16; ++i) o[i] = 0.f;
#pragma unroll
            for (int s = 0; s < 2; ++s) o = MFMA32(__builtin_bit_cast(bf16x8, va[et][s]), pf[s], o);
            o = MFMA32(__builtin_bit_cast(bf16x8, hh == 0 ? vm[et] : zero4), pf[2], o);
#pragma unroll
            for (int g4 = 0; g4 < 4; ++g4) {
                v2u v = {pack2(o[4 * g4], o[4 * g4 + 1]), pack2(o[4 * g4 + 2], o[4 * g4 + 3])};
                *(v2u*)(so + (sg * 32 + r) * OS + et * 32 + 8 * g4 + 4 * hh) = v;
            }
        }
    }
    __syncthreads();
    TR(5)
    auto rP = __builtin_amdgcn_make_buffer_rsrc(ws + OFF_PART + ((size_t)h * S_ + G * 128) * D_ * 4, 0, 128 * D_ * 4, 0x00020000);
#pragma unroll
    for (int rnd = 0; rnd < 2; ++rnd) {
        const int dt = wave + 8 * rnd;
        if (dt < 14) {
            f32x16 acc[4];
#pragma unroll
            for (int b = 0; b < 4; ++b)
#pragma unroll
                for (int i = 0; i < 16; ++i) acc[b][i] = 0.f;
            bf16x8 wc[16];
#pragma unroll
            for (int ks = 0; ks < 16; ++ks) wc[ks] = wf[ks];
            if (rnd == 1) { TR(6) }
            if (rnd == 0 && wave + 8 < 14) {
#pragma unroll
                for (int ks = 0; ks < 16; ++ks) wf[ks] = *(const bf16x8*)(WoF + ((size_t)((wave + 8) * 16 + ks) * 64 + lane) * 8);
            }
#pragma unroll
            for (int ks = 0; ks < 16; ++ks) {
#pragma unroll
                for (int b = 0; b < 4; ++b) {
                    bf16x8 of = *(const bf16x8*)(so + (b * 32 + r) * OS + ks * 16 + hh * 8);
                    acc[b] = MFMA32(of, wc[ks], acc[b]);
                }
            }
#pragma unroll
            for (int b = 0; b < 4; ++b)
#pragma unroll
                for (int reg = 0; reg < 16; ++reg) {
                    int t = b * 32 + (reg & 3) + 8 * (reg >> 2) + 4 * hh;
                    __builtin_amdgcn_raw_buffer_store_b32(__float_as_uint(acc[b][reg]), rP, (t * D_ + dt * 32 + r) * 4, 0, 16);
                }
        }
    }
}

DI void layernorm8(const float (&x)[8], float (&o)[8], bool on) {
    float s = 0.f;
#pragma unroll
    for (int i = 0; i < 8; ++i) s += x[i];
    s = wave_sum(s);
    const float mean = s * (1.0f / 448.0f);
    float q = 0.f;
#pragma unroll
    for (int i = 0; i < 8; ++i) { float dlt = x[i] - mean; q += on ? dlt * dlt : 0.f; }
    q = wave_sum(q);
    const float m = rb(mean), v = rb(q * (1.0f / 448.0f));
    const float den = rb(sqrtf(rb(v + 1e-5f)));
#pragma unroll
    for (int i = 0; i < 8; ++i) o[i] = on ? rb(rb(x[i] - m) / den) : 0.f;
}
DI void unpack8(v4u w, float (&x)[8]) {
    x[0] = __uint_as_float(w.x << 16); x[1] = __uint_as_float(w.x & 0xffff0000u); x[2] = __uint_as_float(w.y << 16); x[3] = __uint_as_float(w.y & 0xffff0000u);
    x[4] = __uint_as_float(w.z << 16); x[5] = __uint_as_float(w.z & 0xffff0000u); x[6] = __uint_as_float(w.w << 16); x[7] = __uint_as_float(w.w & 0xffff0000u);
}
DI v4u pack8(const float (&x)[8]) { v4u w = {pack2(x[0], x[1]), pack2(x[2], x[3]), pack2(x[4], x[5]), pack2(x[6], x[7])}; return w; }
DI void task_red(char* ws, u16* __restrict__ out, int layer, int rb8, int tid) {
    const int lane = tid & 63, wave = tid >> 6;
    const bool on = lane < 56;
    const int lc = on ? lane : 0;
    auto rP = __builtin_amdgcn_make_buffer_rsrc(ws + OFF_PART, 0, (unsigned)((size_t)16 * S_ * D_ * 4), 0x00020000);
    auto rX = __builtin_amdgcn_make_buffer_rsrc(ws + (layer == 0 ? OFF_X1 : OFF_X3), 0, S_ * D_ * 2, 0x00020000);
    auto rX3 = __builtin_amdgcn_make_buffer_rsrc(ws + OFF_X3, 0, S_ * D_ * 2, 0x00020000);
    auto rH3 = __builtin_amdgcn_make_buffer_rsrc(ws + OFF_HB + (size_t)S_ * D_ * 2, 0, S_ * D_ * 2, 0x00020000);
    const u16* yr = (const u16*)(ws + OFF_Y);
    const int t = rb8 * 8 + wave;
    f32x4 pa[16], pb[16];
#pragma unroll
    for (int h = 0; h < 16; ++h) {
        pa[h] = __builtin_bit_cast(f32x4, __builtin_amdgcn_raw_buffer_load_b128(rP, ((h * S_ + t) * D_ + lc * 8) * 4, 0, 16));
        pb[h] = __builtin_bit_cast(f32x4, __builtin_amdgcn_raw_buffer_load_b128(rP, ((h * S_ + t) * D_ + lc * 8 + 4) * 4, 0, 16));
    }
    v4u xw = __builtin_amdgcn_raw_buffer_load_b128(rX, (t * D_ + lc * 8) * 2, 0, 16);
    v4u yw = *(const v4u*)(yr + (size_t)t * D_ + lc * 8);
    float x[8], ln[8], y[8];
    unpack8(xw, x); unpack8(yw, y);
#pragma unroll
    for (int i = 0; i < 4; ++i) {
        float sa = 0.f, sb = 0.f;
#pragma unroll
        for (int h = 0; h < 16; ++h) { sa += pa[h][i]; sb += pb[h][i]; }
        x[i] = on ? rb(x[i] + rb(sa)) : 0.f; x[4 + i] = on ? rb(x[4 + i] + rb(sb)) : 0.f;
    }
    if (layer == 1) {
        if (on) *(v4u*)(out + (size_t)t * D_ + lane * 8) = pack8(x);
    } else {
        layernorm8(x, ln, on);
#pragma unroll
        for (int i = 0; i < 8; ++i) x[i] = on ? rb(x[i] + rb(ln[i] * y[i])) : 0.f;
        layernorm8(x, ln, on);
        if (on) {
            __builtin_amdgcn_raw_buffer_store_b128(pack8(x), rX3, (t * D_ + lane * 8) * 2, 0, 16);
            __builtin_amdgcn_raw_buffer_store_b128(pack8(ln), rH3, (t * D_ + lane * 8) * 2, 0, 16);
        }
    }
}

DI void chain_role(const u16* __restrict__ mem0, char* __restrict__ ws, char* smem, int start1, int nticks) {
    u16* sMem = (u16*)smem;
    u16* sQ = sMem + 16 * MS;
    u16* sKm = sQ + 16 * 72;
    u16* sVT = sKm + 16 * 72;
    float* sS = (float*)(sVT + 256 * 24);
    u16* sO = (u16*)(sS + 16 * SSR);
    u16* sM = sO + 16 * 264;
    const int wg = blockIdx.x, tid = threadIdx.x, lane = tid & 63;
    const int wave = __builtin_amdgcn_readfirstlane(tid >> 6);
    const int h = wg >> 2, g = wg & 3;
    const int r16 = lane & 15, kg = lane >> 4;
    const int start0 = 0;
    const u16* WT = (const u16*)(ws + OFF_WQKVT);
    const u16* WoT = (const u16*)(ws + OFF_WOT);
    auto rK = __builtin_amdgcn_make_buffer_rsrc(ws + OFF_K, 0, (unsigned)((size_t)2 * S_ * NH_ * 2), 0x00020000);
    auto rV = __builtin_amdgcn_make_buffer_rsrc(ws + OFF_VT, 0, (unsigned)((size_t)2 * S_ * NH_ * 2), 0x00020000);
    auto rKM = __builtin_amdgcn_make_buffer_rsrc(ws + OFF_KMEM, 0, (unsigned)((size_t)2 * NSEG_ * 8 * NH_ * 2), 0x00020000);
    auto rVM = __builtin_amdgcn_make_buffer_rsrc(ws + OFF_VMEMT, 0, (unsigned)((size_t)2 * NSEG_ * 8 * NH_ * 2), 0x00020000);
    unsigned* errw = flag_ptr(ws, FL_ERR);
    auto rA = __builtin_amdgcn_make_buffer_rsrc(ws + OFF_XA, 0, (unsigned)SZ_XA, 0x00020000);
    auto rB = __builtin_amdgcn_make_buffer_rsrc(ws + OFF_XB, 0, (unsigned)SZ_XB, 0x00020000);
    auto rC = __builtin_amdgcn_make_buffer_rsrc(ws + OFF_XC, 0, (unsigned)SZ_XC, 0x00020000);
    const v4u sent4 = {SENT, SENT, SENT, SENT};
    const bf16x8 zero8 = {0, 0, 0, 0, 0, 0, 0, 0};
    const v4u zero4 = {0u, 0u, 0u, 0u};

    bf16x8 wreg[30];
    {
        const int w4 = wave & 3;
        const int m0 = wave < 4 ? 0 : 2;
#pragma unroll
        for (int ks = 0; ks < 14; ++ks)
            wreg[ks] = *(const bf16x8*)(WT + (size_t)(m0 * 4096 + h * DH_ + g * 64 + w4 * 16 + r16) * D_ + ks * 32 + kg * 8);
        if (wave < 4) {
#pragma unroll
            for (int ks = 0; ks < 14; ++ks)
                wreg[14 + ks] = *(const bf16x8*)(WT + (size_t)(1 * 4096 + h * DH_ + g * 64 + w4 * 16 + r16) * D_ + ks * 32 + kg * 8);
            wreg[28] = zero8; wreg[29] = zero8;
        } else {
#pragma unroll
            for (int tl = 0; tl < 2; ++tl)
#pragma unroll
                for (int ks = 0; ks < 8; ++ks) {
                    int dt = 2 * w4 + tl;
                    wreg[14 + tl * 8 + ks] = (dt < 7) ? *(const bf16x8*)(WoT + (size_t)(g * 112 + dt * 16 + r16) * NH_ + h * DH_ + ks * 32 + kg * 8) : zero8;
                }
        }
    }
    for (int i = tid; i < 16 * MS; i += 512) sMem[i] = 0;
    for (int i = tid; i < 256 * 24; i += 512) sVT[i] = 0;

    bf16x8 kxf[2] = {zero8, zero8};
    bf16x8 vxf[2][2];
    auto load_kx = [&](int T) {
        if (wave >= 1 && wave <= 4) {
            const int c = (wave - 1) >> 1, hf = (wave - 1) & 1;
            int tc = T - (c ? start1 : start0);
            tc = tc < 0 ? 0 : (tc > NSEG_ - 1 ? NSEG_ - 1 : tc);
#pragma unroll
            for (int ks = 0; ks < 2; ++ks)
                kxf[ks] = __builtin_bit_cast(bf16x8, __builtin_amdgcn_raw_buffer_load_b128(rK, (unsigned)(((((c * NSEG_ + tc) * 16 + h) * 32 + g * 8 + ks * 4 + kg) * 32 + hf * 16 + r16) * 16), 0, 16));
        }
    };
    auto load_vx = [&](int T) {
#pragma unroll
        for (int c = 0; c < 2; ++c) {
            int tc = T - (c ? start1 : start0);
            tc = tc < 0 ? 0 : (tc > NSEG_ - 1 ? NSEG_ - 1 : tc);
#pragma unroll
            for (int tl = 0; tl < 2; ++tl)
                vxf[c][tl] = __builtin_bit_cast(bf16x8, __builtin_amdgcn_raw_buffer_load_b128(rV, (unsigned)(((((c * NSEG_ + tc) * 16 + h) * 4 + kg) * 256 + (wave * 2 + tl) * 16 + r16) * 16), 0, 16));
        }
    };
    auto wait_qkv = [&](int T) {
#pragma unroll
        for (int c = 0; c < 2; ++c) {
            const int tc = T - (c ? start1 : start0);
            if (tc >= 0 && tc < NSEG_ && (tc & 3) == 0) {
                int sp = 0;
                for (; sp < WAIT_CAP; ++sp) {
                    if (flag_load(ws, FL_QKV + (c * 16 + (tc >> 2)) * 16 + h) >= 6u) break;
                    if (flag_load(ws, FL_ERR) != 0u) break;
                    __builtin_amdgcn_s_sleep(2);
                }
                if (sp == WAIT_CAP) __hip_atomic_store(errw, 6u, __ATOMIC_RELAXED, __HIP_MEMORY_SCOPE_AGENT);
            }
        }
    };
    wait_qkv(0);
    load_kx(0); load_vx(0);
    __syncthreads();

    for (int T = 0; T < nticks; ++T) {
        const int sl = T & (RING - 1), sn = (T + 1) & (RING - 1);
        const int tc0 = T - start0, tc1 = T - start1;
        const bool act0 = tc0 >= 0 && tc0 < NSEG_;
        const bool act1 = tc1 >= 0 && tc1 < NSEG_;
        const unsigned cOff = (unsigned)(((sl * NCH + wg) * 896 + tid) * 16);
        if (tid < 448) {
            v4u v0 = zero4, v1 = zero4;
            if (T > 0) {
                int sp = 0;
                for (; sp < SPIN_CAP; ++sp) {
                    asm volatile("" ::: "memory");
                    v0 = __builtin_amdgcn_raw_buffer_load_b128(rC, cOff, 0, 16);
                    v1 = __builtin_amdgcn_raw_buffer_load_b128(rC, cOff + 448 * 16, 0, 16);
                    if (okv(v0) && okv(v1)) break;
                }
                if (sp == SPIN_CAP) __hip_atomic_store(errw, 1u, __ATOMIC_RELAXED, __HIP_MEMORY_SCOPE_AGENT);
            }
            const int row = tid / 56, c = tid % 56;
            if (!act0) v0 = zero4;
            if (!act1) v1 = zero4;
            if ((act0 && tc0 == 0) || (act1 && tc1 == 0)) {
                v4u m = *(const v4u*)(mem0 + (size_t)row * D_ + c * 8);
                if (act0 && tc0 == 0) v0 = m;
                if (act1 && tc1 == 0) v1 = m;
            }
            *(v4u*)(sMem + row * MS + c * 8) = v0;
            *(v4u*)(sMem + (8 + row) * MS + c * 8) = v1;
        }
        __syncthreads();
        STAMP(0)
        const int cr = r16 >> 3;
        if (wave < 4) {
            f32x4 aq = {0.f, 0.f, 0.f, 0.f}, ak = {0.f, 0.f, 0.f, 0.f};
#pragma unroll
            for (int ks = 0; ks < 14; ++ks) {
                bf16x8 mf = *(const bf16x8*)(sMem + r16 * MS + ks * 32 + kg * 8);
                aq = MFMA16(wreg[ks], mf, aq);
                ak = MFMA16(wreg[14 + ks], mf, ak);
            }
            v2u vq = {pack2(aq[0], aq[1]), pack2(aq[2], aq[3])};
            v2u vk = {pack2(ak[0], ak[1]), pack2(ak[2], ak[3])};
            *(v2u*)(sQ + r16 * 72 + wave * 16 + 4 * kg) = vq;
            *(v2u*)(sKm + r16 * 72 + wave * 16 + 4 * kg) = vk;
            if (cr ? act1 : act0) {
                const int tc = cr ? tc1 : tc0;
                __builtin_amdgcn_raw_buffer_store_b64(vk, rKM, (unsigned)(((((cr * NSEG_ + tc) * 16 + h) * 32 + g * 8 + wave * 2 + (kg >> 1)) * 8 + (r16 & 7)) * 16 + (kg & 1) * 8), 0, 16);
            }
        } else {
            const int w4 = wave - 4;
            f32x4 av = {0.f, 0.f, 0.f, 0.f};
#pragma unroll
            for (int ks = 0; ks < 14; ++ks) {
                bf16x8 mf = *(const bf16x8*)(sMem + r16 * MS + ks * 32 + kg * 8);
                av = MFMA16(mf, wreg[ks], av);
            }
            v2u vv = {san(pack2(av[0], av[1])), san(pack2(av[2], av[3]))};
            const int e = w4 * 16 + r16;
#pragma unroll
            for (int c = 0; c < GG; ++c)
                __builtin_amdgcn_raw_buffer_store_b64(vv, rA, (unsigned)((((sl * NCH + h * GG + c) * GG + g) * XA_UNITS + 320) * 16 + e * 32 + kg * 8), 0, 16);
            const int ck = kg >> 1;
            if (ck ? act1 : act0) {
                const int tc = ck ? tc1 : tc0;
                __builtin_amdgcn_raw_buffer_store_b64(vv, rVM, (unsigned)((((ck * NSEG_ + tc) * NH_ + h * DH_ + g * 64 + e) * 8 + 4 * (kg & 1)) * 2), 0, 16);
            }
        }
        __syncthreads();
        STAMP(1)
        const bool svalid = (wave == 0) ? ((kg >> 1) == cr) : (((wave - 1) >> 1) == cr);
        if (wave < 5) {
            f32x4 s = {0.f, 0.f, 0.f, 0.f};
#pragma unroll
            for (int ks = 0; ks < 2; ++ks) {
                bf16x8 qf = *(const bf16x8*)(sQ + r16 * 72 + ks * 32 + kg * 8);
                bf16x8 kf = (((wave - 1) >> 1) ? act1 : act0) ? kxf[ks] : zero8;
                if (wave == 0) { v4u kl = *(const v4u*)(sKm + r16 * 72 + ks * 32 + kg * 8); asm volatile("" : "+v"(kl.x), "+v"(kl.y), "+v"(kl.z), "+v"(kl.w)); kf = __builtin_bit_cast(bf16x8, kl); }
                s = MFMA16(kf, qf, s);
            }
            v4u v = san4(__builtin_bit_cast(v4u, s));
            if (svalid) {
#pragma unroll
                for (int c = 0; c < GG; ++c)
                    __builtin_amdgcn_raw_buffer_store_b128(v, rA, (unsigned)((((sl * NCH + h * GG + c) * GG + g) * XA_UNITS + wave * 64 + lane) * 16), 0, 16);
            }
        }
        if (T > 0 && tid < 448) {
            __builtin_amdgcn_raw_buffer_store_b128(sent4, rC, cOff, 0, 16);
            __builtin_amdgcn_raw_buffer_store_b128(sent4, rC, cOff + 448 * 16, 0, 16);
        }
        STAMP(2)
        const unsigned aOffS = (unsigned)(((sl * NCH + wg) * GG * XA_UNITS + tid) * 16);
        const int vu = tid - 320;
        auto aOffV = [&](int k) { const int x = vu + 192 * k; return (unsigned)((((sl * NCH + wg) * GG + (x >> 7)) * XA_UNITS + 320 + (x & 127)) * 16); };
        const bool v3 = vu < 128;
        if (wave < 5) {
            if (svalid) {
                v4u v[GG];
                int sp = 0;
                for (; sp < SPIN_CAP; ++sp) {
                    asm volatile("" ::: "memory");
                    bool ok = true;
#pragma unroll
                    for (int p = 0; p < GG; ++p) v[p] = __builtin_amdgcn_raw_buffer_load_b128(rA, aOffS + p * XA_UNITS * 16, 0, 16);
#pragma unroll
                    for (int p = 0; p < GG; ++p) ok = ok && okv(v[p]);
                    if (ok) break;
                }
                if (sp == SPIN_CAP) __hip_atomic_store(errw, 2u, __ATOMIC_RELAXED, __HIP_MEMORY_SCOPE_AGENT);
                f32x4 s = __builtin_bit_cast(f32x4, v[0]);
#pragma unroll
                for (int p = 1; p < GG; ++p) s += __builtin_bit_cast(f32x4, v[p]);
                const int col = (wave == 0) ? (kg & 1) * 4 : 8 + ((wave - 1) & 1) * 16 + kg * 4;
                *(f32x4*)(sS + r16 * SSR + col) = s;
            }
        } else {
            v4u v0, v1, v2 = zero4;
            int sp = 0;
            for (; sp < SPIN_CAP; ++sp) {
                asm volatile("" ::: "memory");
                v0 = __builtin_amdgcn_raw_buffer_load_b128(rA, aOffV(0), 0, 16);
                v1 = __builtin_amdgcn_raw_buffer_load_b128(rA, aOffV(1), 0, 16);
                if (v3) v2 = __builtin_amdgcn_raw_buffer_load_b128(rA, aOffV(2), 0, 16);
                if (okv(v0) && okv(v1) && okv(v2)) break;
            }
            if (sp == SPIN_CAP) __hip_atomic_store(errw, 3u, __ATOMIC_RELAXED, __HIP_MEMORY_SCOPE_AGENT);
            { const int x = vu; *(v4u*)(sVT + (x >> 1) * 24 + (x & 1) * 8) = v0; }
            { const int x = vu + 192; *(v4u*)(sVT + (x >> 1) * 24 + (x & 1) * 8) = v1; }
            if (v3) { const int x = vu + 384; *(v4u*)(sVT + (x >> 1) * 24 + (x & 1) * 8) = v2; }
        }
        __syncthreads();
        STAMP(3)
        if (tid == 0) {
            if (act0) flag_add(ws, FL_MEM + (0 * 16 + (tc0 >> 2)) * 16 + h);
            if (act1) flag_add(ws, FL_MEM + (1 * 16 + (tc1 >> 2)) * 16 + h);
        }
        {
            float xs[8], ms[8];
            {
                f32x4 a = *(const f32x4*)(sS + r16 * SSR + 8 + kg * 8), b = *(const f32x4*)(sS + r16 * SSR + 12 + kg * 8);
                f32x4 c = *(const f32x4*)(sS + r16 * SSR), d = *(const f32x4*)(sS + r16 * SSR + 4);
#pragma unroll
                for (int j = 0; j < 4; ++j) { xs[j] = a[j]; xs[4 + j] = b[j]; ms[j] = c[j]; ms[4 + j] = d[j]; }
            }
            const bool hasm = kg == cr;
            float mx = -3.0e38f;
#pragma unroll
            for (int j = 0; j < 8; ++j) {
                xs[j] = rb(xs[j]) * 0.0625f; mx = fmaxf(mx, xs[j]);
                ms[j] = rb(ms[j]) * 0.0625f; mx = fmaxf(mx, ms[j]);
            }
            mx = xmax16(mx); mx = xmax32(mx);
            float sum = 0.f, summ = 0.f;
#pragma unroll
            for (int j = 0; j < 8; ++j) { xs[j] = __expf(xs[j] - mx); sum += xs[j]; ms[j] = __expf(ms[j] - mx); summ += ms[j]; }
            sum = xsum16(sum); sum = xsum32(sum);
            const float inv = 1.0f / (sum + summ);
            v4u pxu, pmu;
            pxu.x = pack2(xs[0] * inv, xs[1] * inv); pxu.y = pack2(xs[2] * inv, xs[3] * inv); pxu.z = pack2(xs[4] * inv, xs[5] * inv); pxu.w = pack2(xs[6] * inv, xs[7] * inv);
            pmu.x = pack2(ms[0] * inv, ms[1] * inv); pmu.y = pack2(ms[2] * inv, ms[3] * inv); pmu.z = pack2(ms[4] * inv, ms[5] * inv); pmu.w = pack2(ms[6] * inv, ms[7] * inv);
            const bf16x8 px = __builtin_bit_cast(bf16x8, pxu);
            const bf16x8 pm = hasm ? __builtin_bit_cast(bf16x8, pmu) : zero8;
            const bf16x8 px0 = cr == 0 ? px : zero8, px1 = cr == 1 ? px : zero8;
#pragma unroll
            for (int tl = 0; tl < 2; ++tl) {
                const int et = wave * 2 + tl;
                bf16x8 vm = zero8;
                if (kg < 2) vm = *(const bf16x8*)(sVT + (et * 16 + r16) * 24 + kg * 8);
                f32x4 o = {0.f, 0.f, 0.f, 0.f};
                o = MFMA16(vm, pm, o);
                o = MFMA16(act0 ? vxf[0][tl] : zero8, px0, o);
                o = MFMA16(act1 ? vxf[1][tl] : zero8, px1, o);
                v2u v = {pack2(o[0], o[1]), pack2(o[2], o[3])};
                *(v2u*)(sO + r16 * 264 + et * 16 + 4 * kg) = v;
            }
        }
        wait_qkv(T + 1);
        load_kx(T + 1); load_vx(T + 1);
        __syncthreads();
        STAMP(4)
        if (T + 1 < nticks) {
            if (wave >= 4) {
                const int w4 = wave - 4;
#pragma unroll
                for (int tl = 0; tl < 2; ++tl) {
                    const int dt = 2 * w4 + tl;
                    if (dt < 7) {
                        f32x4 a = {0.f, 0.f, 0.f, 0.f};
#pragma unroll
                        for (int ks = 0; ks < 8; ++ks) {
                            bf16x8 of = *(const bf16x8*)(sO + r16 * 264 + ks * 32 + kg * 8);
                            a = MFMA16(wreg[14 + tl * 8 + ks], of, a);
                        }
                        v4u v = san4(__builtin_bit_cast(v4u, a));
                        __builtin_amdgcn_raw_buffer_store_b128(v, rB, (unsigned)((((sl * NCH + r16 * GG + g) * 16 + h) * 32 + dt * 4 + kg) * 16), 0, 16);
                    }
                }
            }
        }
        if (wave < 5) {
            if (svalid) {
#pragma unroll
                for (int p = 0; p < GG; ++p) __builtin_amdgcn_raw_buffer_store_b128(sent4, rA, aOffS + p * XA_UNITS * 16, 0, 16);
            }
        } else {
            __builtin_amdgcn_raw_buffer_store_b128(sent4, rA, aOffV(0), 0, 16);
            __builtin_amdgcn_raw_buffer_store_b128(sent4, rA, aOffV(1), 0, 16);
            if (v3) __builtin_amdgcn_raw_buffer_store_b128(sent4, rA, aOffV(2), 0, 16);
        }
        STAMP(5)
        if (T + 1 < nticks) {
            if (wave < 7) {
                const int p = r16, u = wave * 4 + kg;
                const unsigned bOff = (unsigned)((((sl * NCH + wg) * 16 + p) * 32 + u) * 16);
                v4u v;
                int sp = 0;
                for (; sp < SPIN_CAP; ++sp) {
                    asm volatile("" ::: "memory");
                    v = __builtin_amdgcn_raw_buffer_load_b128(rB, bOff, 0, 16);
                    if (okv(v)) break;
                }
                if (sp == SPIN_CAP) __hip_atomic_store(errw, 4u, __ATOMIC_RELAXED, __HIP_MEMORY_SCOPE_AGENT);
                float s0 = rowsum16(__uint_as_float(v.x)), s1 = rowsum16(__uint_as_float(v.y)), s2 = rowsum16(__uint_as_float(v.z)), s3 = rowsum16(__uint_as_float(v.w));
                v2u o = {san(pack2(s0, s1)), san(pack2(s2, s3))};
                if (p == 0) *(v2u*)(sM + u * 4) = o;
                __builtin_amdgcn_raw_buffer_store_b128(sent4, rB, bOff, 0, 16);
            }
            __syncthreads();
            STAMP(6)
            for (int i = tid; i < NCH * 14; i += 512) {
                int c = i / 14, k = i % 14;
                v4u v = *(const v4u*)(sM + k * 8);
                __builtin_amdgcn_raw_buffer_store_b128(v, rC, (unsigned)(((sn * NCH + c) * 896 + h * 56 + g * 14 + k) * 16), 0, 16);
            }
        }
        STAMP(7)
    }
}

constexpr int LAG = 8;
constexpr int DGRP = 2;
constexpr int NT_I = 16 * 96;
constexpr int NT_G = 16 + 16 + 96 + 16 + 16;
constexpr int NT_ALL = NT_I + (16 + DGRP) * NT_G;

__global__ void __launch_bounds__(512, 2) k_mega(const u16* __restrict__ mem0, char* __restrict__ ws_, u16* __restrict__ out_) {
    __shared__ __attribute__((aligned(16))) char smem[LDS_BYTES];
    __shared__ int s_ticket;
    if (blockIdx.x < NCH) { chain_role(mem0, ws_, smem, LAG, NSEG_ + LAG); return; }
    for (;;) {
        __syncthreads();
        if (threadIdx.x == 0) s_ticket = (int)__hip_atomic_fetch_add(flag_ptr(ws_, FL_TICKET), 1u, __ATOMIC_RELAXED, __HIP_MEMORY_SCOPE_AGENT);
        __syncthreads();
        int tk = __builtin_amdgcn_readfirstlane(s_ticket);
        if (tk >= NT_ALL) break;
        char* ws = ws_; asm volatile("" : "+s"(ws));
#define TRC nullptr
        u16* out = out_; asm volatile("" : "+s"(out));
        int tid = threadIdx.x; asm volatile("" : "+v"(tid));
        if (tk < NT_I) {
            const int G = tk / 96, j = tk % 96, nb = (j + 32) % 96;
            TR(1) task_qkv(ws, smem, 0, G, nb, tid); TR(2)
            signal_flag(ws, FL_QKV + (0 * 16 + G) * 16 + ((nb >> 1) & 15));
            continue;
        }
        tk -= NT_I;
        const int G = tk / NT_G; int j = tk % NT_G;
        if (j < 16) {
            if (G < 16) {
                wait_flag(ws, FL_QKV + (0 * 16 + G) * 16 + j, 6u);
                wait_flag(ws, FL_MEM + (0 * 16 + G) * 16 + j, 16u);
                TR(1) task_att(ws, smem, 0, G, j, tid, TRC); TR(2)
                signal_flag(ws, FL_ATT + 0 * 16 + G);
            }
            continue;
        }
        j -= 16;
        if (j < 16) {
            if (G < 16) {
                wait_flag(ws, FL_ATT + 0 * 16 + G, 16u);
                TR(1) task_red(ws, out, 0, G * 16 + j, tid); TR(2)
                signal_flag(ws, FL_RED + 0 * 16 + G);
            }
            continue;
        }
        j -= 16;
        if (j < 96) {
            if (G < 16) {
                const int nb = (j + 32) % 96;
                wait_flag(ws, FL_RED + 0 * 16 + G, 16u);
                TR(1) task_qkv(ws, smem, 1, G, nb, tid); TR(2)
                signal_flag(ws, FL_QKV + (1 * 16 + G) * 16 + ((nb >> 1) & 15));
            }
            continue;
        }
        j -= 96;
        const int G1 = G - DGRP;
        if (G1 < 0) continue;
        if (j < 16) {
            wait_flag(ws, FL_QKV + (1 * 16 + G1) * 16 + j, 6u);
            wait_flag(ws, FL_MEM + (1 * 16 + G1) * 16 + j, 16u);
            TR(1) task_att(ws, smem, 1, G1, j, tid, TRC); TR(2)
            signal_flag(ws, FL_ATT + 1 * 16 + G1);
            continue;
        }
        j -= 16;
        wait_flag(ws, FL_ATT + 1 * 16 + G1, 16u);
        TR(1) task_red(ws, out, 1, G1 * 16 + j, tid); TR(2)
    }
}

extern "C" void kernel_launch(void* const* d_in, const int* in_sizes, int n_in, void* d_out, int out_size, void* d_ws, size_t ws_size, hipStream_t stream) {
    const u16* A_q = (const u16*)d_in[0];
    const u16* A_k = (const u16*)d_in[1];
    const u16* A_v = (const u16*)d_in[2];
    const u16* A_shk = (const u16*)d_in[3];
    const u16* A_ssmk = (const u16*)d_in[4];
    const u16* A_ssmD = (const u16*)d_in[5];
    const u16* B_mem0 = (const u16*)d_in[7];
    const u16* B_Wq = (const u16*)d_in[8];
    const u16* B_Wk = (const u16*)d_in[9];
    const u16* B_Wv = (const u16*)d_in[10];
    const u16* B_Wo = (const u16*)d_in[11];
    char* ws = (char*)d_ws;
    u16* out = (u16*)d_out;

    (void)hipMemsetAsync(ws + OFF_XA, 0xFF, OFF_FL - OFF_XA, stream);
    (void)hipMemsetAsync(ws + OFF_FL, 0, SZ_FL, stream);
    k_prep<<<3 * 448 + 448 + 224, 256, 0, stream>>>(B_Wq, B_Wk, B_Wv, B_Wo, A_k, A_v, A_shk, ws);
    k_conv<<<448, 256, 0, stream>>>(A_ssmk, A_ssmD, ws);
    k_stage1<<<64, 256, 0, stream>>>(A_q, ws);
    k_mega<<<256, 512, 0, stream>>>(B_mem0, ws, out);
}
```

```cpp
#include <hip/hip_runtime.h>
#include <hip/hip_bf16.h>
#include <stdint.h>

constexpr int S_ = 2048, D_ = 448, H_ = 16, DH_ = 256, NH_ = 4096, SEG_ = 32, NSEG_ = 64, MM_ = 8;

typedef unsigned short u16;
typedef __attribute__((ext_vector_type(8))) short bf16x8;
typedef __attribute__((ext_vector_type(4))) float f32x4;
typedef __attribute__((ext_vector_type(16))) float f32x16;
typedef unsigned v4u __attribute__((ext_vector_type(4)));
typedef unsigned v2u __attribute__((ext_vector_type(2)));

#define DI __device__ __forceinline__
#define MFMA32(a, b, c) __builtin_amdgcn_mfma_f32_32x32x16_bf16((a), (b), (c), 0, 0, 0)
#define MFMA16(a, b, c) __builtin_amdgcn_mfma_f32_16x16x32_bf16((a), (b), (c), 0, 0, 0)

DI float bf2f(u16 v) { return __uint_as_float(((unsigned)v) << 16); }
typedef __attribute__((ext_vector_type(2))) __bf16 bf16x2_t;
typedef __attribute__((ext_vector_type(2))) float f32x2;
DI unsigned pack2(float a, float b) { f32x2 f = {a, b}; return __builtin_bit_cast(unsigned, __builtin_convertvector(f, bf16x2_t)); }
DI u16 f2bf(float f) { return (u16)(pack2(f, 0.f) & 0xffffu); }
DI float rb(float f) { return __uint_as_float(pack2(f, 0.f) << 16); }

constexpr size_t al(size_t x) { return (x + 255) & ~(size_t)255; }
constexpr size_t OFF_WQKVT = 0;
constexpr size_t OFF_WOT   = OFF_WQKVT + al((size_t)12288 * 448 * 2);
constexpr size_t OFF_WOF   = OFF_WOT + al((size_t)448 * 4096 * 2);
constexpr size_t OFF_KVT   = OFF_WOF + al((size_t)448 * 4096 * 2);
constexpr size_t OFF_YT    = OFF_KVT + al((size_t)448 * 2048 * 2);
constexpr size_t OFF_Y     = OFF_YT + al((size_t)448 * 2048 * 2);
constexpr size_t OFF_X1    = OFF_Y + al((size_t)448 * 2048 * 2);
constexpr size_t OFF_X3    = OFF_X1 + al((size_t)2048 * 448 * 2);
constexpr size_t OFF_HB    = OFF_X3 + al((size_t)2048 * 448 * 2);
constexpr size_t OFF_Q     = OFF_HB + al((size_t)2 * 2048 * 448 * 2);
constexpr size_t OFF_K     = OFF_Q + al((size_t)2 * 2048 * 4096 * 2);
constexpr size_t OFF_VT    = OFF_K + al((size_t)2 * 2048 * 4096 * 2);
constexpr size_t OFF_KMEM  = OFF_VT + al((size_t)2 * 2048 * 4096 * 2);
constexpr size_t OFF_VMEMT = OFF_KMEM + al((size_t)2 * 64 * 8 * 4096 * 2);
constexpr size_t OFF_PART  = OFF_VMEMT + al((size_t)2 * 64 * 8 * 4096 * 2);
constexpr int RING = 4, NCH = 64, GG = 4;
constexpr int XA_UNITS = 320 + 128;
constexpr size_t OFF_XA    = OFF_PART + al((size_t)16 * 2048 * 448 * 4);
constexpr size_t SZ_XA     = (size_t)RING * NCH * GG * XA_UNITS * 16;
constexpr size_t OFF_XB    = OFF_XA + al(SZ_XA);
constexpr size_t SZ_XB     = (size_t)RING * NCH * 16 * 32 * 16;
constexpr size_t OFF_XC    = OFF_XB + al(SZ_XB);
constexpr size_t SZ_XC     = (size_t)RING * NCH * 896 * 16;
constexpr size_t OFF_FL    = OFF_XC + al(SZ_XC);
constexpr int FL_TICKET = 0, FL_ERR = 1, FL_QKV = 16, FL_MEM = FL_QKV + 512, FL_ATT = FL_MEM + 512, FL_RED = FL_ATT + 32, FL_N = FL_RED + 32;
constexpr size_t SZ_FL     = (size_t)FL_N * 64;
constexpr size_t WS_END    = OFF_FL + al(SZ_FL);

__device__ void transpose_tile(const u16* __restrict__ src, int C, int r0, int c0, u16* __restrict__ dst, int ldd, int dr0, u16 (*tile)[66]) {
    const int tid = threadIdx.x;
    for (int i = tid; i < 64 * 8; i += 256) {
        int r = i >> 3, cq = i & 7;
        v4u v = *(const v4u*)(src + (size_t)(r0 + r) * C + c0 + cq * 8);
        unsigned w[4] = {v.x, v.y, v.z, v.w};
#pragma unroll
        for (int j = 0; j < 4; ++j) { tile[r][cq * 8 + 2 * j] = (u16)(w[j] & 0xffff); tile[r][cq * 8 + 2 * j + 1] = (u16)(w[j] >> 16); }
    }
    __syncthreads();
    for (int i = tid; i < 64 * 8; i += 256) {
        int c = i >> 3, rq = i & 7;
        unsigned w[4];
#pragma unroll
        for (int j = 0; j < 4; ++j) w[j] = (unsigned)tile[rq * 8 + 2 * j][c] | ((unsigned)tile[rq * 8 + 2 * j + 1][c] << 16);
        v4u v = {w[0], w[1], w[2], w[3]};
        *(v4u*)(dst + (size_t)(dr0 + c0 + c) * ldd + r0 + rq * 8) = v;
    }
    __syncthreads();
}

__global__ void __launch_bounds__(256) k_prep(const u16* __restrict__ Wq, const u16* __restrict__ Wk, const u16* __restrict__ Wv, const u16* __restrict__ Wo,
                                               const u16* __restrict__ kin, const u16* __restrict__ vin, const u16* __restrict__ shk, char* __restrict__ ws) {
    __shared__ u16 tile[64][66];
    int b = blockIdx.x;
    if (b < 3 * 448) {
        int w = b / 448, t = b % 448;
        const u16* src = w == 0 ? Wq : (w == 1 ? Wk : Wv);
        int r0 = (t / 64) * 64, c0 = (t % 64) * 64;
        transpose_tile(src, 4096, r0, c0, (u16*)(ws + OFF_WQKVT), 448, w * 4096, tile);
        return;
    }
    b -= 3 * 448;
    if (b < 448) {
        int r0 = (b / 7) * 64, c0 = (b % 7) * 64;
        transpose_tile(Wo, 448, r0, c0, (u16*)(ws + OFF_WOT), 4096, 0, tile);
        u16* wof = (u16*)(ws + OFF_WOF);
        for (int i = threadIdx.x; i < 64 * 8; i += 256) {
            int dl = i & 63, eq = i >> 6;
            int e0 = r0 + eq * 8, d = c0 + dl;
            int h = e0 >> 8, ee = e0 & 255, ks = ee >> 4, hh = (ee >> 3) & 1, dt = d >> 5, r = d & 31;
            unsigned w[4];
#pragma unroll
            for (int j = 0; j < 4; ++j) w[j] = (unsigned)tile[eq * 8 + 2 * j][dl] | ((unsigned)tile[eq * 8 + 2 * j + 1][dl] << 16);
            v4u v = {w[0], w[1], w[2], w[3]};
            *(v4u*)(wof + ((((size_t)h * 14 + dt) * 16 + ks) * 64 + hh * 32 + r) * 8) = v;
        }
        return;
    }
    b -= 448;
    {
        int t0 = (b / 7) * 64, d0 = (b % 7) * 64;
        const int tid = threadIdx.x;
        for (int i = tid; i < 64 * 64; i += 256) {
            int tt = i >> 6, dd = i & 63;
            int t = t0 + tt, d = d0 + dd;
            float acc = 0.f;
#pragma unroll
            for (int j = 0; j < 4; ++j) {
                int ts = t + j - 3;
                float kv_ = ts >= 0 ? bf2f(kin[(size_t)ts * D_ + d]) : 0.f;
                float pr = rb(kv_ * bf2f(shk[d * 4 + (3 - j)]));
                acc = rb(acc + pr);
            }
            float kvv = rb(acc * bf2f(vin[(size_t)t * D_ + d]));
            tile[dd][tt] = f2bf(kvv);
        }
        __syncthreads();
        u16* kvT = (u16*)(ws + OFF_KVT);
        for (int i = tid; i < 64 * 32; i += 256) {
            int dd = i >> 5, tp = i & 31;
            unsigned w = (unsigned)tile[dd][2 * tp] | ((unsigned)tile[dd][2 * tp + 1] << 16);
            *(unsigned*)(kvT + (size_t)(d0 + dd) * S_ + t0 + 2 * tp) = w;
        }
    }
}

constexpr int CV_GS = 2088;
__global__ void __launch_bounds__(256) k_conv(const u16* __restrict__ ssmk, const u16* __restrict__ ssmD, char* __restrict__ ws) {
    __shared__ __attribute__((aligned(16))) u16 gcp[8 * CV_GS];
    __shared__ __attribute__((aligned(16))) u16 kvs[96 * 40];
    __shared__ float ysum[2048];
    const int d = blockIdx.x, tid = threadIdx.x, lane = tid & 63, wave = tid >> 6;
    const u16* hrow = ssmk + (size_t)d * S_;
    const u16* kvT = (const u16*)(ws + OFF_KVT) + (size_t)d * S_;
    for (int i = tid; i < 8 * CV_GS; i += 256) {
        int p = i / CV_GS, x = i % CV_GS;
        int m = x + p;
        u16 v = 0;
        if (m <= 2047) v = hrow[2047 - m];
        gcp[i] = v;
    }
    for (int i = tid; i < 96 * 40; i += 256) {
        int blk = i / 40, j = i % 40;
        u16 v = 0;
        if (blk >= 32 && j < 32) v = kvT[(blk - 32) * 32 + j];
        kvs[i] = v;
    }
    for (int i = tid; i < 2048; i += 256) ysum[i] = 0.f;
    __syncthreads();
    f32x16 acc0, acc1;
#pragma unroll
    for (int i = 0; i < 16; ++i) { acc0[i] = 0.f; acc1[i] = 0.f; }
    const int r = lane & 31, hh = lane >> 5;
    for (int dl = wave; dl < 64; dl += 4) {
#pragma unroll
        for (int ks = 0; ks < 2; ++ks) {
            int a = 32 * dl + r - 16 * ks - 8 * hh;
            int base = 2047 - a;
            int p = base & 7, q = base >> 3;
            bf16x8 af = *(const bf16x8*)(gcp + p * CV_GS + q * 8);
            {
                int blk = 32 + (32 + r - dl);
                bf16x8 bfrag = *(const bf16x8*)(kvs + blk * 40 + 16 * ks + 8 * hh);
                acc1 = MFMA32(af, bfrag, acc1);
            }
            if (dl < 32) {
                int blk = 32 + (r - dl);
                bf16x8 bfrag = *(const bf16x8*)(kvs + blk * 40 + 16 * ks + 8 * hh);
                acc0 = MFMA32(af, bfrag, acc0);
            }
        }
    }
#pragma unroll
    for (int reg = 0; reg < 16; ++reg) {
        int j = (reg & 3) + 8 * (reg >> 2) + 4 * hh;
        atomicAdd(&ysum[32 * r + j], acc0[reg]);
        atomicAdd(&ysum[32 * (32 + r) + j], acc1[reg]);
    }
    __syncthreads();
    const float Dd = bf2f(ssmD[d]);
    u16* yT = (u16*)(ws + OFF_YT) + (size_t)d * S_;
    for (int t = tid; t < 2048; t += 256) {
        float kvv = bf2f(kvs[(32 + (t >> 5)) * 40 + (t & 31)]);
        float y = rb(rb(ysum[t]) + rb(kvv * Dd));
        yT[t] = f2bf(y);
    }
}

DI float wave_sum(float v) {
#pragma unroll
    for (int m = 32; m >= 1; m >>= 1) v += __shfl_xor(v, m);
    return v;
}
DI void layernorm7(const float (&x)[7], float (&o)[7]) {
    float s = 0.f;
#pragma unroll
    for (int i = 0; i < 7; ++i) s += x[i];
    s = wave_sum(s);
    const float mean = s * (1.0f / 448.0f);
    float q = 0.f;
#pragma unroll
    for (int i = 0; i < 7; ++i) { float dlt = x[i] - mean; q += dlt * dlt; }
    q = wave_sum(q);
    const float m = rb(mean), v = rb(q * (1.0f / 448.0f));
    const float den = rb(sqrtf(rb(v + 1e-5f)));
#pragma unroll
    for (int i = 0; i < 7; ++i) o[i] = rb(rb(x[i] - m) / den);
}

__global__ void __launch_bounds__(256) k_stage1(const u16* __restrict__ x0, char* __restrict__ ws) {
    __shared__ u16 yt[32][450];
    const int t0 = blockIdx.x * 32, tid = threadIdx.x, lane = tid & 63, wave = tid >> 6;
    const u16* yT = (const u16*)(ws + OFF_YT);
    for (int i = tid; i < 448 * 16; i += 256) {
        int d = i >> 4, tp = i & 15;
        unsigned w = *(const unsigned*)(yT + (size_t)d * S_ + t0 + 2 * tp);
        yt[2 * tp][d] = (u16)(w & 0xffff); yt[2 * tp + 1][d] = (u16)(w >> 16);
    }
    __syncthreads();
    u16* x1 = (u16*)(ws + OFF_X1); u16* hb = (u16*)(ws + OFF_HB); u16* yr = (u16*)(ws + OFF_Y);
    for (int rr = wave; rr < 32; rr += 4) {
        int t = t0 + rr;
        float x[7], ln[7];
#pragma unroll
        for (int i = 0; i < 7; ++i) x[i] = bf2f(x0[(size_t)t * D_ + lane + 64 * i]);
        layernorm7(x, ln);
#pragma unroll
        for (int i = 0; i < 7; ++i) x[i] = rb(x[i] + rb(ln[i] * bf2f(yt[rr][lane + 64 * i])));
        layernorm7(x, ln);
#pragma unroll
        for (int i = 0; i < 7; ++i) {
            x1[(size_t)t * D_ + lane + 64 * i] = f2bf(x[i]); hb[(size_t)t * D_ + lane + 64 * i] = f2bf(ln[i]);
            yr[(size_t)t * D_ + lane + 64 * i] = yt[rr][lane + 64 * i];
        }
    }
}

constexpr unsigned SENT = 0xFFFFFFFFu;
DI bool okv(v4u v) { return v.x != SENT && v.y != SENT && v.z != SENT && v.w != SENT; }
DI unsigned san(unsigned w) { return w == SENT ? 0x7FC07FC0u : w; }
DI v4u san4(v4u v) { v4u o = {san(v.x), san(v.y), san(v.z), san(v.w)}; return o; }
constexpr int SPIN_CAP = 1 << 16;
constexpr int WAIT_CAP = 1 << 16;
constexpr int MS = 456;
constexpr int SSR = 44;
constexpr int GS = 72;
constexpr int OS = 264;
constexpr int LDS_BYTES = 80 * 1024;

DI float xmax16(float v) { auto r = __builtin_amdgcn_permlane16_swap(__float_as_uint(v), __float_as_uint(v), false, false); return fmaxf(__uint_as_float(r[0]), __uint_as_float(r[1])); }
DI float xmax32(float v) { auto r = __builtin_amdgcn_permlane32_swap(__float_as_uint(v), __float_as_uint(v), false, false); return fmaxf(__uint_as_float(r[0]), __uint_as_float(r[1])); }
DI float xsum16(float v) { auto r = __builtin_amdgcn_permlane16_swap(__float_as_uint(v), __float_as_uint(v), false, false); return __uint_as_float(r[0]) + __uint_as_float(r[1]); }
DI float xsum32(float v) { auto r = __builtin_amdgcn_permlane32_swap(__float_as_uint(v), __float_as_uint(v), false, false); return __uint_as_float(r[0]) + __uint_as_float(r[1]); }
template <int CTRL> DI float dpp_f(float v) { return __uint_as_float(__builtin_amdgcn_update_dpp(0u, __float_as_uint(v), CTRL, 0xF, 0xF, false)); }
DI float rowsum16(float v) {
    v += dpp_f<0x128>(v);
    v += dpp_f<0x124>(v);
    v += dpp_f<0x122>(v);
    v += dpp_f<0x121>(v);
    return v;
}

#define TR(i)
#define STAMP(i)

#define PSTAG 12
#define POLL2(N, v, LDSTMT, ERRCODE) { \
    v4u pa_[N], pb_[N]; \
    { v4u (&d_)[N] = pa_; LDSTMT } \
    __builtin_amdgcn_s_sleep(PSTAG); \
    int sp_ = 0; \
    for (;;) { \
        asm volatile("" ::: "memory"); \
        { v4u (&d_)[N] = pb_; LDSTMT } \
        asm volatile("" ::: "memory"); \
        { bool ok_ = true; _Pragma("unroll") for (int i_ = 0; i_ < N; ++i_) ok_ = ok_ && okv(pa_[i_]); \
          if (__all(ok_)) { _Pragma("unroll") for (int i_ = 0; i_ < N; ++i_) v[i_] = pa_[i_]; break; } } \
        asm volatile("" ::: "memory"); \
        { v4u (&d_)[N] = pa_; LDSTMT } \
        asm volatile("" ::: "memory"); \
        { bool ok_ = true; _Pragma("unroll") for (int i_ = 0; i_ < N; ++i_) ok_ = ok_ && okv(pb_[i_]); \
          if (__all(ok_)) { _Pragma("unroll") for (int i_ = 0; i_ < N; ++i_) v[i_] = pb_[i_]; break; } } \
        if (++sp_ >= SPIN_CAP) { __hip_atomic_store(errw, ERRCODE, __ATOMIC_RELAXED, __HIP_MEMORY_SCOPE_AGENT); _Pragma("unroll") for (int i_ = 0; i_ < N; ++i_) v[i_] = pa_[i_]; break; } \
    } }

DI unsigned* flag_ptr(char* ws, int idx) { return (unsigned*)(ws + OFF_FL + (size_t)idx * 64); }
DI unsigned flag_load(char* ws, int idx) { return __hip_atomic_load(flag_ptr(ws, idx), __ATOMIC_RELAXED, __HIP_MEMORY_SCOPE_AGENT); }
DI void flag_add(char* ws, int idx) { __hip_atomic_fetch_add(flag_ptr(ws, idx), 1u, __ATOMIC_RELAXED, __HIP_MEMORY_SCOPE_AGENT); }
DI void wait_flag(char* ws, int idx, unsigned target) {
    if (threadIdx.x == 0) {
        int sp = 0;
        for (; sp < WAIT_CAP; ++sp) {
            if (flag_load(ws, idx) >= target) break;
            if (flag_load(ws, FL_ERR) != 0u) break;
            __builtin_amdgcn_s_sleep(8);
        }
        if (sp == WAIT_CAP) __hip_atomic_store(flag_ptr(ws, FL_ERR), 5u, __ATOMIC_RELAXED, __HIP_MEMORY_SCOPE_AGENT);
    }
    __syncthreads();
}
DI void signal_flag(char* ws, int idx) {
    __builtin_amdgcn_s_waitcnt(0x0F70);
    __syncthreads();
    if (threadIdx.x == 0) flag_add(ws, idx);
}

DI void task_qkv(char* ws, char* smem, int layer, int G, int nb, int tid) {
    u16* sW = (u16*)smem;
    u16* sH = sW + 2 * 128 * GS;
    const int lane = tid & 63, wave = tid >> 6;
    const int n0 = nb * 128, t0 = G * 128;
    const u16* WT = (const u16*)(ws + OFF_WQKVT) + (size_t)n0 * D_;
    auto rH = __builtin_amdgcn_make_buffer_rsrc(ws + OFF_HB + ((size_t)layer * S_ + t0) * D_ * 2, 0, 128 * D_ * 2, 0x00020000);
    const bool isV = nb >= 64;
    const int wr = wave >> 1, wc = wave & 1;
    const int r = lane & 31, hh = lane >> 5;
    f32x16 acc[2];
#pragma unroll
    for (int b = 0; b < 2; ++b)
#pragma unroll
        for (int i = 0; i < 16; ++i) acc[b][i] = 0.f;
    v4u pw[7][2], ph[7][2];
#pragma unroll
    for (int kb = 0; kb < 7; ++kb)
#pragma unroll
        for (int i = 0; i < 2; ++i) {
            int c = tid + 512 * i, row = c >> 3, kq = c & 7;
            ph[kb][i] = __builtin_amdgcn_raw_buffer_load_b128(rH, (row * D_ + kb * 64 + kq * 8) * 2, 0, 16);
            pw[kb][i] = *(const v4u*)(WT + (size_t)row * D_ + kb * 64 + kq * 8);
        }
#pragma unroll
    for (int i = 0; i < 2; ++i) { int c = tid + 512 * i, row = c >> 3, kq = c & 7; *(v4u*)(sW + row * GS + kq * 8) = pw[0][i]; *(v4u*)(sH + row * GS + kq * 8) = ph[0][i]; }
    __syncthreads();
#pragma unroll
    for (int kb = 0; kb < 7; ++kb) {
        const int cur = (kb & 1) * 128 * GS, nxt = ((kb + 1) & 1) * 128 * GS;
        if (kb < 6) {
#pragma unroll
            for (int i = 0; i < 2; ++i) { int c = tid + 512 * i, row = c >> 3, kq = c & 7; *(v4u*)(sW + nxt + row * GS + kq * 8) = pw[kb + 1][i]; *(v4u*)(sH + nxt + row * GS + kq * 8) = ph[kb + 1][i]; }
        }
#pragma unroll
        for (int ks = 0; ks < 4; ++ks) {
            bf16x8 fw = *(const bf16x8*)(sW + cur + (wr * 32 + r) * GS + ks * 16 + hh * 8);
            bf16x8 fh[2];
#pragma unroll
            for (int b = 0; b < 2; ++b) fh[b] = *(const bf16x8*)(sH + cur + (wc * 64 + b * 32 + r) * GS + ks * 16 + hh * 8);
            if (!isV) {
#pragma unroll
                for (int b = 0; b < 2; ++b) acc[b] = MFMA32(fw, fh[b], acc[b]);
            } else {
#pragma unroll
                for (int b = 0; b < 2; ++b) acc[b] = MFMA32(fh[b], fw, acc[b]);
            }
        }
        __syncthreads();
    }
    u16* sT = (u16*)smem;
    if (!isV) {
#pragma unroll
        for (int b = 0; b < 2; ++b) {
            int t = wc * 64 + b * 32 + r;
#pragma unroll
            for (int g4 = 0; g4 < 4; ++g4) {
                int n = wr * 32 + 8 * g4 + 4 * hh;
                v2u v = {pack2(acc[b][4 * g4], acc[b][4 * g4 + 1]), pack2(acc[b][4 * g4 + 2], acc[b][4 * g4 + 3])};
                *(v2u*)(sT + t * 136 + n) = v;
            }
        }
        __syncthreads();
        auto rD = __builtin_amdgcn_make_buffer_rsrc(ws + (nb < 32 ? OFF_Q : OFF_K) + ((size_t)layer * S_ + t0) * NH_ * 2, 0, 128 * NH_ * 2, 0x00020000);
        const int hd = (nb >> 1) & 15, c0 = (nb & 1) * 16;
#pragma unroll
        for (int k = 0; k < 4; ++k) {
            int i = tid + 512 * k, sg = i >> 9, c = (i >> 5) & 15, row = i & 31;
            v4u v = *(const v4u*)(sT + (sg * 32 + row) * 136 + c * 8);
            __builtin_amdgcn_raw_buffer_store_b128(v, rD, (((sg * 16 + hd) * 32 + c0 + c) * 32 + row) * 16, 0, 16);
        }
    } else {
#pragma unroll
        for (int b = 0; b < 2; ++b) {
            int n = wr * 32 + r;
#pragma unroll
            for (int g4 = 0; g4 < 4; ++g4) {
                int t = wc * 64 + b * 32 + 8 * g4 + 4 * hh;
                v2u v = {pack2(acc[b][4 * g4], acc[b][4 * g4 + 1]), pack2(acc[b][4 * g4 + 2], acc[b][4 * g4 + 3])};
                *(v2u*)(sT + n * 136 + t) = v;
            }
        }
        __syncthreads();
        auto rD = __builtin_amdgcn_make_buffer_rsrc(ws + OFF_VT + ((size_t)layer * S_ + t0) * NH_ * 2, 0, 128 * NH_ * 2, 0x00020000);
        const int hd = (nb >> 1) & 15, e0 = (nb & 1) * 128;
#pragma unroll
        for (int k = 0; k < 4; ++k) {
            int i = tid + 512 * k, sg = i >> 9, kc = (i >> 7) & 3, e = i & 127;
            v4u v = *(const v4u*)(sT + e * 136 + sg * 32 + kc * 8);
            __builtin_amdgcn_raw_buffer_store_b128(v, rD, (((sg * 16 + hd) * 4 + kc) * 256 + e0 + e) * 16, 0, 16);
        }
    }
}

DI void task_att(char* ws, char* smem, int layer, int G, int h, int tid, unsigned long long* trc) {
    u16* so = (u16*)smem;
    v4u* sP = (v4u*)(smem + 128 * OS * 2);
    const int lane = tid & 63, wave = tid >> 6;
    const int sg = wave & 3, role = wave >> 2;
    const int seg = G * 4 + sg;
    const int r = lane & 31, hh = lane >> 5;
    const int pr = (r & 19) | ((r & 4) << 1) | ((r & 8) >> 1);
    const size_t sh = ((size_t)layer * NSEG_ + seg) * 16 + h;
    const u16* WoF = (const u16*)(ws + OFF_WOF) + (size_t)h * 14 * 8192;
    const v4u zero4 = {0u, 0u, 0u, 0u};
    bf16x8 wf[16];
    v4u va[8][2], vm[8];
    if (role == 0) {
        auto rQ = __builtin_amdgcn_make_buffer_rsrc(ws + OFF_Q + sh * 8192 * 2, 0, 8192 * 2, 0x00020000);
        auto rK = __builtin_amdgcn_make_buffer_rsrc(ws + OFF_K + sh * 8192 * 2, 0, 8192 * 2, 0x00020000);
        auto rKM = __builtin_amdgcn_make_buffer_rsrc(ws + OFF_KMEM + sh * 2048 * 2, 0, 2048 * 2, 0x00020000);
        f32x16 s1, s0;
#pragma unroll
        for (int i = 0; i < 16; ++i) { s1[i] = 0.f; s0[i] = 0.f; }
        v4u qf[16], kf[16], mf[16];
#pragma unroll
        for (int ks = 0; ks < 16; ++ks) {
            qf[ks] = __builtin_amdgcn_raw_buffer_load_b128(rQ, ((2 * ks + hh) * 32 + r) * 16, 0, 16);
            kf[ks] = __builtin_amdgcn_raw_buffer_load_b128(rK, ((2 * ks + hh) * 32 + pr) * 16, 0, 16);
            mf[ks] = __builtin_amdgcn_raw_buffer_load_b128(rKM, ((2 * ks + hh) * 8 + (pr & 7)) * 16, 0, 16);
        }
#pragma unroll
        for (int ks = 0; ks < 16; ++ks) {
            s1 = MFMA32(__builtin_bit_cast(bf16x8, kf[ks]), __builtin_bit_cast(bf16x8, qf[ks]), s1);
            v4u m = pr < 8 ? mf[ks] : zero4;
            s0 = MFMA32(__builtin_bit_cast(bf16x8, m), __builtin_bit_cast(bf16x8, qf[ks]), s0);
        }
        TR(3)
        float sc[16], sm[8];
        float mx = -3.0e38f;
#pragma unroll
        for (int reg = 0; reg < 16; ++reg) {
            int key = 16 * (reg >> 3) + 8 * hh + (reg & 7);
            float v = rb(s1[reg]) * 0.0625f;
            v = (key <= r) ? v : -1000000000.0f;
            sc[reg] = v; mx = fmaxf(mx, v);
        }
#pragma unroll
        for (int reg = 0; reg < 8; ++reg) { float v = rb(s0[reg]) * 0.0625f; sm[reg] = v; if (hh == 0) mx = fmaxf(mx, v); }
        mx = xmax32(mx);
        float sum = 0.f;
#pragma unroll
        for (int reg = 0; reg < 16; ++reg) { sc[reg] = __expf(sc[reg] - mx); sum += sc[reg]; }
#pragma unroll
        for (int reg = 0; reg < 8; ++reg) { sm[reg] = hh == 0 ? __expf(sm[reg] - mx) : 0.f; sum += sm[reg]; }
        sum = xsum32(sum);
        const float inv = 1.0f / sum;
#pragma unroll
        for (int s = 0; s < 2; ++s) {
            v4u w = {pack2(sc[8 * s] * inv, sc[8 * s + 1] * inv), pack2(sc[8 * s + 2] * inv, sc[8 * s + 3] * inv), pack2(sc[8 * s + 4] * inv, sc[8 * s + 5] * inv), pack2(sc[8 * s + 6] * inv, sc[8 * s + 7] * inv)};
            sP[(sg * 3 + s) * 64 + lane] = w;
        }
        { v4u w = {pack2(sm[0] * inv, sm[1] * inv), pack2(sm[2] * inv, sm[3] * inv), pack2(sm[4] * inv, sm[5] * inv), pack2(sm[6] * inv, sm[7] * inv)}; sP[(sg * 3 + 2) * 64 + lane] = w; }
        TR(4)
    } else {
        auto rV = __builtin_amdgcn_make_buffer_rsrc(ws + OFF_VT + sh * 8192 * 2, 0, 8192 * 2, 0x00020000);
        auto rVM = __builtin_amdgcn_make_buffer_rsrc(ws + OFF_VMEMT + sh * 2048 * 2, 0, 2048 * 2, 0x00020000);
#pragma unroll
        for (int et = 0; et < 8; ++et) {
#pragma unroll
            for (int s = 0; s < 2; ++s) va[et][s] = __builtin_amdgcn_raw_buffer_load_b128(rV, ((2 * s + hh) * 256 + et * 32 + r) * 16, 0, 16);
            vm[et] = __builtin_amdgcn_raw_buffer_load_b128(rVM, (et * 32 + r) * 16, 0, 16);
        }
    }
#pragma unroll
    for (int ks = 0; ks < 16; ++ks) wf[ks] = *(const bf16x8*)(WoF + ((size_t)(wave * 16 + ks) * 64 + lane) * 8);
    __syncthreads();
    if (role == 1) {
        bf16x8 pf[3];
#pragma unroll
        for (int s = 0; s < 3; ++s) pf[s] = __builtin_bit_cast(bf16x8, sP[(sg * 3 + s) * 64 + lane]);
#pragma unroll
        for (int et = 0; et < 8; ++et) {
            f32x16 o;
#pragma unroll
            for (int i = 0; i < 16; ++i) o[i] = 0.f;
#pragma unroll
            for (int s = 0; s < 2; ++s) o = MFMA32(__builtin_bit_cast(bf16x8, va[et][s]), pf[s], o);
            o = MFMA32(__builtin_bit_cast(bf16x8, hh == 0 ? vm[et] : zero4), pf[2], o);
#pragma unroll
            for (int g4 = 0; g4 < 4; ++g4) {
                v2u v = {pack2(o[4 * g4], o[4 * g4 + 1]), pack2(o[4 * g4 + 2], o[4 * g4 + 3])};
                *(v2u*)(so + (sg * 32 + r) * OS + et * 32 + 8 * g4 + 4 * hh) = v;
            }
        }
    }
    __syncthreads();
    TR(5)
    auto rP = __builtin_amdgcn_make_buffer_rsrc(ws + OFF_PART + ((size_t)h * S_ + G * 128) * D_ * 4, 0, 128 * D_ * 4, 0x00020000);
#pragma unroll
    for (int rnd = 0; rnd < 2; ++rnd) {
        const int dt = wave + 8 * rnd;
        if (dt < 14) {
            f32x16 acc[4];
#pragma unroll
            for (int b = 0; b < 4; ++b)
#pragma unroll
                for (int i = 0; i < 16; ++i) acc[b][i] = 0.f;
            bf16x8 wc[16];
#pragma unroll
            for (int ks = 0; ks < 16; ++ks) wc[ks] = wf[ks];
            if (rnd == 1) { TR(6) }
            if (rnd == 0 && wave + 8 < 14) {
#pragma unroll
                for (int ks = 0; ks < 16; ++ks) wf[ks] = *(const bf16x8*)(WoF + ((size_t)((wave + 8) * 16 + ks) * 64 + lane) * 8);
            }
#pragma unroll
            for (int ks = 0; ks < 16; ++ks) {
#pragma unroll
                for (int b = 0; b < 4; ++b) {
                    bf16x8 of = *(const bf16x8*)(so + (b * 32 + r) * OS + ks * 16 + hh * 8);
                    acc[b] = MFMA32(of, wc[ks], acc[b]);
                }
            }
#pragma unroll
            for (int b = 0; b < 4; ++b)
#pragma unroll
                for (int reg = 0; reg < 16; ++reg) {
                    int t = b * 32 + (reg & 3) + 8 * (reg >> 2) + 4 * hh;
                    __builtin_amdgcn_raw_buffer_store_b32(__float_as_uint(acc[b][reg]), rP, (t * D_ + dt * 32 + r) * 4, 0, 16);
                }
        }
    }
}

DI void layernorm8(const float (&x)[8], float (&o)[8], bool on) {
    float s = 0.f;
#pragma unroll
    for (int i = 0; i < 8; ++i) s += x[i];
    s = wave_sum(s);
    const float mean = s * (1.0f / 448.0f);
    float q = 0.f;
#pragma unroll
    for (int i = 0; i < 8; ++i) { float dlt = x[i] - mean; q += on ? dlt * dlt : 0.f; }
    q = wave_sum(q);
    const float m = rb(mean), v = rb(q * (1.0f / 448.0f));
    const float den = rb(sqrtf(rb(v + 1e-5f)));
#pragma unroll
    for (int i = 0; i < 8; ++i) o[i] = on ? rb(rb(x[i] - m) / den) : 0.f;
}
DI void unpack8(v4u w, float (&x)[8]) {
    x[0] = __uint_as_float(w.x << 16); x[1] = __uint_as_float(w.x & 0xffff0000u); x[2] = __uint_as_float(w.y << 16); x[3] = __uint_as_float(w.y & 0xffff0000u);
    x[4] = __uint_as_float(w.z << 16); x[5] = __uint_as_float(w.z & 0xffff0000u); x[6] = __uint_as_float(w.w << 16); x[7] = __uint_as_float(w.w & 0xffff0000u);
}
DI v4u pack8(const float (&x)[8]) { v4u w = {pack2(x[0], x[1]), pack2(x[2], x[3]), pack2(x[4], x[5]), pack2(x[6], x[7])}; return w; }
DI void task_red(char* ws, u16* __restrict__ out, int layer, int rb8, int tid) {
    const int lane = tid & 63, wave = tid >> 6;
    const bool on = lane < 56;
    const int lc = on ? lane : 0;
    auto rP = __builtin_amdgcn_make_buffer_rsrc(ws + OFF_PART, 0, (unsigned)((size_t)16 * S_ * D_ * 4), 0x00020000);
    auto rX = __builtin_amdgcn_make_buffer_rsrc(ws + (layer == 0 ? OFF_X1 : OFF_X3), 0, S_ * D_ * 2, 0x00020000);
    auto rX3 = __builtin_amdgcn_make_buffer_rsrc(ws + OFF_X3, 0, S_ * D_ * 2, 0x00020000);
    auto rH3 = __builtin_amdgcn_make_buffer_rsrc(ws + OFF_HB + (size_t)S_ * D_ * 2, 0, S_ * D_ * 2, 0x00020000);
    const u16* yr = (const u16*)(ws + OFF_Y);
    const int t = rb8 * 8 + wave;
    f32x4 pa[16], pb[16];
#pragma unroll
    for (int h = 0; h < 16; ++h) {
        pa[h] = __builtin_bit_cast(f32x4, __builtin_amdgcn_raw_buffer_load_b128(rP, ((h * S_ + t) * D_ + lc * 8) * 4, 0, 16));
        pb[h] = __builtin_bit_cast(f32x4, __builtin_amdgcn_raw_buffer_load_b128(rP, ((h * S_ + t) * D_ + lc * 8 + 4) * 4, 0, 16));
    }
    v4u xw = __builtin_amdgcn_raw_buffer_load_b128(rX, (t * D_ + lc * 8) * 2, 0, 16);
    v4u yw = *(const v4u*)(yr + (size_t)t * D_ + lc * 8);
    float x[8], ln[8], y[8];
    unpack8(xw, x); unpack8(yw, y);
#pragma unroll
    for (int i = 0; i < 4; ++i) {
        float sa = 0.f, sb = 0.f;
#pragma unroll
        for (int h = 0; h < 16; ++h) { sa += pa[h][i]; sb += pb[h][i]; }
        x[i] = on ? rb(x[i] + rb(sa)) : 0.f; x[4 + i] = on ? rb(x[4 + i] + rb(sb)) : 0.f;
    }
    if (layer == 1) {
        if (on) *(v4u*)(out + (size_t)t * D_ + lane * 8) = pack8(x);
    } else {
        layernorm8(x, ln, on);
#pragma unroll
        for (int i = 0; i < 8; ++i) x[i] = on ? rb(x[i] + rb(ln[i] * y[i])) : 0.f;
        layernorm8(x, ln, on);
        if (on) {
            __builtin_amdgcn_raw_buffer_store_b128(pack8(x), rX3, (t * D_ + lane * 8) * 2, 0, 16);
            __builtin_amdgcn_raw_buffer_store_b128(pack8(ln), rH3, (t * D_ + lane * 8) * 2, 0, 16);
        }
    }
}

#define BLD(r, vo, so) __builtin_amdgcn_raw_buffer_load_b128((r), (vo), (so), 16)
#define BST(v, r, vo, so) __builtin_amdgcn_raw_buffer_store_b128((v), (r), (vo), (so), 16)
#define BST64(v, r, vo, so) __builtin_amdgcn_raw_buffer_store_b64((v), (r), (vo), (so), 16)
DI void chain_role(const u16* __restrict__ mem0, char* __restrict__ ws, char* smem, int start1, int nticks) {
    u16* sMem = (u16*)smem;
    u16* sQ = sMem + 16 * MS;
    u16* sKm = sQ + 16 * 72;
    u16* sVT = sKm + 16 * 72;
    float* sS = (float*)(sVT + 256 * 24);
    u16* sO = (u16*)(sS + 16 * SSR);
    u16* sM = sO + 16 * 264;
    const int wg = blockIdx.x, tid = threadIdx.x, lane = tid & 63;
    const int wave = __builtin_amdgcn_readfirstlane(tid >> 6);
    const int h = wg >> 2, g = wg & 3;
    const int r16 = lane & 15, kg = lane >> 4;
    const int start0 = 0;
    const u16* WT = (const u16*)(ws + OFF_WQKVT);
    const u16* WoT = (const u16*)(ws + OFF_WOT);
    auto rX = __builtin_amdgcn_make_buffer_rsrc(ws + OFF_XA, 0, (unsigned)(OFF_FL - OFF_XA), 0x00020000);
    auto rD = __builtin_amdgcn_make_buffer_rsrc(ws + OFF_K, 0, (unsigned)(OFF_PART - OFF_K), 0x00020000);
    constexpr unsigned XB0 = (unsigned)(OFF_XB - OFF_XA), XC0 = (unsigned)(OFF_XC - OFF_XA);
    constexpr unsigned VT0 = (unsigned)(OFF_VT - OFF_K), KM0 = (unsigned)(OFF_KMEM - OFF_K), VM0 = (unsigned)(OFF_VMEMT - OFF_K);
    unsigned* errw = flag_ptr(ws, FL_ERR);
    const v4u sent4 = {SENT, SENT, SENT, SENT};
    const bf16x8 zero8 = {0, 0, 0, 0, 0, 0, 0, 0};
    const v4u zero4 = {0u, 0u, 0u, 0u};

    bf16x8 wreg[30];
    {
        const int w4 = wave & 3;
        const int m0 = wave < 4 ? 0 : 2;
#pragma unroll
        for (int ks = 0; ks < 14; ++ks)
            wreg[ks] = *(const bf16x8*)(WT + (size_t)(m0 * 4096 + h * DH_ + g * 64 + w4 * 16 + r16) * D_ + ks * 32 + kg * 8);
        if (wave < 4) {
#pragma unroll
            for (int ks = 0; ks < 14; ++ks)
                wreg[14 + ks] = *(const bf16x8*)(WT + (size_t)(1 * 4096 + h * DH_ + g * 64 + w4 * 16 + r16) * D_ + ks * 32 + kg * 8);
            wreg[28] = zero8; wreg[29] = zero8;
        } else {
#pragma unroll
            for (int tl = 0; tl < 2; ++tl)
#pragma unroll
                for (int ks = 0; ks < 8; ++ks) {
                    int dt = 2 * w4 + tl;
                    wreg[14 + tl * 8 + ks] = (dt < 7) ? *(const bf16x8*)(WoT + (size_t)(g * 112 + dt * 16 + r16) * NH_ + h * DH_ + ks * 32 + kg * 8) : zero8;
                }
        }
    }
    for (int i = tid; i < 16 * MS; i += 512) sMem[i] = 0;
    for (int i = tid; i < 256 * 24; i += 512) sVT[i] = 0;

    const int cr = r16 >> 3;
    const int ck = kg >> 1;
    const unsigned vT = (unsigned)tid * 16u;
    const int vu = tid - 320;
    const bool v3 = vu < 128;
    unsigned vKV, vAv = 0;
    if (wave < 4) vKV = KM0 + (unsigned)((((cr * NSEG_) * 16 * 32 + wave * 2 + (kg >> 1)) * 8 + (r16 & 7)) * 16 + (kg & 1) * 8);
    else {
        const int e = (wave - 4) * 16 + r16;
        vKV = VM0 + (unsigned)((((ck * NSEG_) * NH_ + e) * 8 + 4 * (kg & 1)) * 2);
        vAv = (unsigned)(320 * 16 + e * 32 + kg * 8);
    }
    unsigned vP0 = 0, vP1 = 0, vP2 = 0;
    if (wave >= 5) {
        auto f = [&](int x) { return (unsigned)(((x >> 7) * XA_UNITS + 320 + (x & 127)) * 16); };
        vP0 = f(vu); vP1 = f(vu + 192); vP2 = v3 ? f(vu + 384) : vP1;
    }
    const unsigned vBs = XB0 + (unsigned)(((r16 * GG) * 16 * 32 + kg) * 16);
    const unsigned vBp = XB0 + (unsigned)((r16 * 32 + wave * 4 + kg) * 16);
    const int pc0 = tid / 14, pk0 = tid % 14, pc1 = (tid + 512) / 14, pk1 = (tid + 512) % 14;
    const unsigned vPub0 = XC0 + (unsigned)((pc0 * 896 + pk0) * 16), vPub1 = XC0 + (unsigned)((pc1 * 896 + pk1) * 16);
    const bool pub1 = tid < NCH * 14 - 512;
    const int kxc = (wave - 1) >> 1;
    const unsigned vKX = (unsigned)((kg * 32 + ((wave - 1) & 1) * 16 + r16) * 16);
    const unsigned vVX = VT0 + (unsigned)((kg * 256 + wave * 32 + r16) * 16);
    const bool svalid = (wave == 0) ? ((kg >> 1) == cr) : (kxc == cr);

    bf16x8 kxf[2] = {zero8, zero8};
    bf16x8 vxf[2][2];
    auto load_x = [&](int T) {
        int t0 = T - start0, t1 = T - start1;
        t0 = t0 < 0 ? 0 : (t0 > NSEG_ - 1 ? NSEG_ - 1 : t0);
        t1 = t1 < 0 ? 0 : (t1 > NSEG_ - 1 ? NSEG_ - 1 : t1);
        const unsigned b0 = (unsigned)((0 * NSEG_ + t0) * 16 + h) * 16384u, b1 = (unsigned)((1 * NSEG_ + t1) * 16 + h) * 16384u;
        if (wave >= 1 && wave <= 4) {
            const unsigned so = (kxc ? b1 : b0) + (unsigned)(g * 8 * 32 * 16);
            kxf[0] = __builtin_bit_cast(bf16x8, BLD(rD, vKX, so));
            kxf[1] = __builtin_bit_cast(bf16x8, BLD(rD, vKX, so + 4 * 32 * 16));
        }
        vxf[0][0] = __builtin_bit_cast(bf16x8, BLD(rD, vVX, b0));
        vxf[0][1] = __builtin_bit_cast(bf16x8, BLD(rD, vVX, b0 + 256));
        vxf[1][0] = __builtin_bit_cast(bf16x8, BLD(rD, vVX, b1));
        vxf[1][1] = __builtin_bit_cast(bf16x8, BLD(rD, vVX, b1 + 256));
    };
    auto wait_qkv = [&](int T) {
#pragma unroll
        for (int c = 0; c < 2; ++c) {
            const int tc = T - (c ? start1 : start0);
            if (tc >= 0 && tc < NSEG_ && (tc & 3) == 0) {
                int sp = 0;
                for (; sp < WAIT_CAP; ++sp) {
                    if (flag_load(ws, FL_QKV + (c * 16 + (tc >> 2)) * 16 + h) >= 6u) break;
                    if (flag_load(ws, FL_ERR) != 0u) break;
                    __builtin_amdgcn_s_sleep(16);
                }
                if (sp == WAIT_CAP) __hip_atomic_store(errw, 6u, __ATOMIC_RELAXED, __HIP_MEMORY_SCOPE_AGENT);
            }
        }
    };
    wait_qkv(0);
    load_x(0);
    __syncthreads();

    for (int T = 0; T < nticks; ++T) {
        const int sl = T & (RING - 1), sn = (T + 1) & (RING - 1), spv = (T - 1) & (RING - 1);
        const int tc0 = T - start0, tc1 = T - start1;
        const bool act0 = tc0 >= 0 && tc0 < NSEG_;
        const bool act1 = tc1 >= 0 && tc1 < NSEG_;
        const unsigned sC = XC0 + (unsigned)((sl * NCH + wg) * 896 * 16);
        const bool doC = T > 0 && tid < 448;
        {
            v4u v0 = zero4, v1 = zero4;
            if (T > 0 && wave < 7) BST(sent4, rX, vBp, (unsigned)((spv * NCH + wg) * 16 * 32 * 16));
            asm volatile("" ::: "memory");
            if (doC) {
                int sp = 0;
                do {
                    asm volatile("" ::: "memory");
                    v0 = BLD(rX, vT, sC);
                    v1 = BLD(rX, vT, sC + 448 * 16);
                    if (++sp == SPIN_CAP) { __hip_atomic_store(errw, 1u, __ATOMIC_RELAXED, __HIP_MEMORY_SCOPE_AGENT); break; }
                } while (!(okv(v0) && okv(v1)));
            }
            if (tid < 448) {
                const int row = tid / 56, c = tid % 56;
                if (!act0) v0 = zero4;
                if (!act1) v1 = zero4;
                if ((act0 && tc0 == 0) || (act1 && tc1 == 0)) {
                    v4u m = *(const v4u*)(mem0 + (size_t)row * D_ + c * 8);
                    if (act0 && tc0 == 0) v0 = m;
                    if (act1 && tc1 == 0) v1 = m;
                }
                *(v4u*)(sMem + row * MS + c * 8) = v0;
                *(v4u*)(sMem + (8 + row) * MS + c * 8) = v1;
            }
        }
        __syncthreads();
        STAMP(0)
        const unsigned sA0 = (unsigned)(((sl * NCH + h * GG) * GG + g) * XA_UNITS * 16);
        v2u kvm;
        if (wave < 4) {
            f32x4 aq = {0.f, 0.f, 0.f, 0.f}, ak = {0.f, 0.f, 0.f, 0.f};
#pragma unroll
            for (int ks = 0; ks < 14; ++ks) {
                bf16x8 mf = *(const bf16x8*)(sMem + r16 * MS + ks * 32 + kg * 8);
                aq = MFMA16(wreg[ks], mf, aq);
                ak = MFMA16(wreg[14 + ks], mf, ak);
            }
            v2u vq = {pack2(aq[0], aq[1]), pack2(aq[2], aq[3])};
            v2u vk = {pack2(ak[0], ak[1]), pack2(ak[2], ak[3])};
            *(v2u*)(sQ + r16 * 72 + wave * 16 + 4 * kg) = vq;
            *(v2u*)(sKm + r16 * 72 + wave * 16 + 4 * kg) = vk;
            kvm = vk;
        } else {
            f32x4 av = {0.f, 0.f, 0.f, 0.f};
#pragma unroll
            for (int ks = 0; ks < 14; ++ks) {
                bf16x8 mf = *(const bf16x8*)(sMem + r16 * MS + ks * 32 + kg * 8);
                av = MFMA16(mf, wreg[ks], av);
            }
            v2u vv = {san(pack2(av[0], av[1])), san(pack2(av[2], av[3]))};
#pragma unroll
            for (int c = 0; c < GG; ++c) BST64(vv, rX, vAv, sA0 + c * GG * XA_UNITS * 16);
            kvm = vv;
        }
        __syncthreads();
        STAMP(1)
        if (wave < 5) {
            f32x4 s = {0.f, 0.f, 0.f, 0.f};
#pragma unroll
            for (int ks = 0; ks < 2; ++ks) {
                bf16x8 qf = *(const bf16x8*)(sQ + r16 * 72 + ks * 32 + kg * 8);
                bf16x8 kf = (kxc ? act1 : act0) ? kxf[ks] : zero8;
                if (wave == 0) { v4u kl = *(const v4u*)(sKm + r16 * 72 + ks * 32 + kg * 8); asm volatile("" : "+v"(kl.x), "+v"(kl.y), "+v"(kl.z), "+v"(kl.w)); kf = __builtin_bit_cast(bf16x8, kl); }
                s = MFMA16(kf, qf, s);
            }
            v4u v = san4(__builtin_bit_cast(v4u, s));
            if (svalid) {
#pragma unroll
                for (int c = 0; c < GG; ++c) BST(v, rX, vT, sA0 + c * GG * XA_UNITS * 16);
            }
        }
        asm volatile("" ::: "memory");
        {
            const unsigned t0o = (unsigned)(tc0 * 16 + h), t1o = (unsigned)(tc1 * 16 + h);
            if (wave < 4) {
                if (cr ? act1 : act0) BST64(kvm, rD, vKV + (cr ? t1o : t0o) * 4096u, (unsigned)(g * 8 * 8 * 16));
            } else {
                if (ck ? act1 : act0) BST64(kvm, rD, vKV + (ck ? tc1 : tc0) * (unsigned)(NH_ * 16), (unsigned)((h * DH_ + g * 64) * 16));
            }
        }
        if (doC) { BST(sent4, rX, vT, sC); BST(sent4, rX, vT, sC + 448 * 16); }
        const int n0 = T + 1 - start0, n1 = T + 1 - start1;
        const bool need0 = n0 >= 0 && n0 < NSEG_ && (n0 & 3) == 0, need1 = n1 >= 0 && n1 < NSEG_ && (n1 & 3) == 0;
        const unsigned fq0 = flag_load(ws, FL_QKV + (0 * 16 + ((n0 >> 2) & 15)) * 16 + h), fq1 = flag_load(ws, FL_QKV + (1 * 16 + ((n1 >> 2) & 15)) * 16 + h);
        STAMP(2)
        const unsigned sAp = (unsigned)((sl * NCH + wg) * GG * XA_UNITS * 16);
        if (wave < 5) {
            if (svalid) {
                v4u v[GG];
                int sp = 0;
                do {
                    asm volatile("" ::: "memory");
#pragma unroll
                    for (int p = 0; p < GG; ++p) v[p] = BLD(rX, vT, sAp + p * XA_UNITS * 16);
                    if (++sp == SPIN_CAP) { __hip_atomic_store(errw, 2u, __ATOMIC_RELAXED, __HIP_MEMORY_SCOPE_AGENT); break; }
                } while (!(okv(v[0]) && okv(v[1]) && okv(v[2]) && okv(v[3])));
                f32x4 s = __builtin_bit_cast(f32x4, v[0]);
#pragma unroll
                for (int p = 1; p < GG; ++p) s += __builtin_bit_cast(f32x4, v[p]);
                const int col = (wave == 0) ? (kg & 1) * 4 : 8 + ((wave - 1) & 1) * 16 + kg * 4;
                *(f32x4*)(sS + r16 * SSR + col) = s;
            }
        } else {
            v4u v0, v1, v2;
            int sp = 0;
            do {
                asm volatile("" ::: "memory");
                v0 = BLD(rX, vP0, sAp); v1 = BLD(rX, vP1, sAp); v2 = BLD(rX, vP2, sAp);
                if (++sp == SPIN_CAP) { __hip_atomic_store(errw, 3u, __ATOMIC_RELAXED, __HIP_MEMORY_SCOPE_AGENT); break; }
            } while (!(okv(v0) && okv(v1) && okv(v2)));
            { const int x = vu; *(v4u*)(sVT + (x >> 1) * 24 + (x & 1) * 8) = v0; }
            { const int x = vu + 192; *(v4u*)(sVT + (x >> 1) * 24 + (x & 1) * 8) = v1; }
            if (v3) { const int x = vu + 384; *(v4u*)(sVT + (x >> 1) * 24 + (x & 1) * 8) = v2; }
        }
        __syncthreads();
        STAMP(3)
        {
            float xs[8], ms[8];
            {
                f32x4 a = *(const f32x4*)(sS + r16 * SSR + 8 + kg * 8), b = *(const f32x4*)(sS + r16 * SSR + 12 + kg * 8);
                f32x4 c = *(const f32x4*)(sS + r16 * SSR), d = *(const f32x4*)(sS + r16 * SSR + 4);
#pragma unroll
                for (int j = 0; j < 4; ++j) { xs[j] = a[j]; xs[4 + j] = b[j]; ms[j] = c[j]; ms[4 + j] = d[j]; }
            }
            const bool hasm = kg == cr;
            float mx = -3.0e38f;
#pragma unroll
            for (int j = 0; j < 8; ++j) {
                xs[j] = rb(xs[j]) * 0.0625f; mx = fmaxf(mx, xs[j]);
                ms[j] = rb(ms[j]) * 0.0625f; mx = fmaxf(mx, ms[j]);
            }
            mx = xmax16(mx); mx = xmax32(mx);
            float sum = 0.f, summ = 0.f;
#pragma unroll
            for (int j = 0; j < 8; ++j) { xs[j] = __expf(xs[j] - mx); sum += xs[j]; ms[j] = __expf(ms[j] - mx); summ += ms[j]; }
            sum = xsum16(sum); sum = xsum32(sum);
            const float inv = 1.0f / (sum + summ);
            v4u pxu, pmu;
            pxu.x = pack2(xs[0] * inv, xs[1] * inv); pxu.y = pack2(xs[2] * inv, xs[3] * inv); pxu.z = pack2(xs[4] * inv, xs[5] * inv); pxu.w = pack2(xs[6] * inv, xs[7] * inv);
            pmu.x = pack2(ms[0] * inv, ms[1] * inv); pmu.y = pack2(ms[2] * inv, ms[3] * inv); pmu.z = pack2(ms[4] * inv, ms[5] * inv); pmu.w = pack2(ms[6] * inv, ms[7] * inv);
            const bf16x8 px = __builtin_bit_cast(bf16x8, pxu);
            const bf16x8 pm = hasm ? __builtin_bit_cast(bf16x8, pmu) : zero8;
            const bf16x8 px0 = cr == 0 ? px : zero8, px1 = cr == 1 ? px : zero8;
#pragma unroll
            for (int tl = 0; tl < 2; ++tl) {
                const int et = wave * 2 + tl;
                bf16x8 vm = zero8;
                if (kg < 2) vm = *(const bf16x8*)(sVT + (et * 16 + r16) * 24 + kg * 8);
                f32x4 o = {0.f, 0.f, 0.f, 0.f};
                o = MFMA16(vm, pm, o);
                o = MFMA16(act0 ? vxf[0][tl] : zero8, px0, o);
                o = MFMA16(act1 ? vxf[1][tl] : zero8, px1, o);
                v2u v = {pack2(o[0], o[1]), pack2(o[2], o[3])};
                *(v2u*)(sO + r16 * 264 + et * 16 + 4 * kg) = v;
            }
        }
        __syncthreads();
        STAMP(4)
        const bool more = T + 1 < nticks;
        if (more && wave >= 4) {
            const int w4 = wave - 4;
            const unsigned sB = (unsigned)((((sl * NCH + g) * 16 + h) * 32 + w4 * 8) * 16);
#pragma unroll
            for (int tl = 0; tl < 2; ++tl) {
                const int dt = 2 * w4 + tl;
                if (dt < 7) {
                    f32x4 a = {0.f, 0.f, 0.f, 0.f};
#pragma unroll
                    for (int ks = 0; ks < 8; ++ks) {
                        bf16x8 of = *(const bf16x8*)(sO + r16 * 264 + ks * 32 + kg * 8);
                        a = MFMA16(wreg[14 + tl * 8 + ks], of, a);
                    }
                    v4u v = san4(__builtin_bit_cast(v4u, a));
                    BST(v, rX, vBs, sB + tl * 64);
                }
            }
        }
        asm volatile("" ::: "memory");
        if (wave < 5) {
            if (svalid) {
#pragma unroll
                for (int q = 0; q < GG; ++q) BST(sent4, rX, vT, sAp + q * XA_UNITS * 16);
            }
        } else {
            BST(sent4, rX, vP0, sAp); BST(sent4, rX, vP1, sAp);
            if (v3) BST(sent4, rX, vP2, sAp);
        }
        STAMP(5)
        {
            const unsigned sBp = (unsigned)((sl * NCH + wg) * 16 * 32 * 16);
            const bool doB = more && wave < 7;
            v4u v = zero4;
            asm volatile("" ::: "memory");
            if (doB) v = BLD(rX, vBp, sBp);
            asm volatile("" ::: "memory");
            if ((need0 && fq0 < 6u) || (need1 && fq1 < 6u)) wait_qkv(T + 1);
            load_x(T + 1);
            asm volatile("" ::: "memory");
            if (doB) {
                int sp = 0;
                while (!okv(v)) {
                    asm volatile("" ::: "memory");
                    v = BLD(rX, vBp, sBp);
                    if (++sp == SPIN_CAP) { __hip_atomic_store(errw, 4u, __ATOMIC_RELAXED, __HIP_MEMORY_SCOPE_AGENT); break; }
                }
                float s0 = rowsum16(__uint_as_float(v.x)), s1 = rowsum16(__uint_as_float(v.y)), s2 = rowsum16(__uint_as_float(v.z)), s3 = rowsum16(__uint_as_float(v.w));
                v2u o = {san(pack2(s0, s1)), san(pack2(s2, s3))};
                if (r16 == 0) *(v2u*)(sM + (wave * 4 + kg) * 4) = o;
            } else {
                __builtin_amdgcn_s_waitcnt(0x0F70);
            }
            __syncthreads();
            STAMP(6)
            if (more) {
                const unsigned sP = (unsigned)((sn * NCH * 896 + h * 56 + g * 14) * 16);
                BST(*(const v4u*)(sM + pk0 * 8), rX, vPub0, sP);
                if (pub1) BST(*(const v4u*)(sM + pk1 * 8), rX, vPub1, sP);
            }
            if (tid == 0) {
                if (act0) flag_add(ws, FL_MEM + (0 * 16 + (tc0 >> 2)) * 16 + h);
                if (act1) flag_add(ws, FL_MEM + (1 * 16 + (tc1 >> 2)) * 16 + h);
            }
        }
        STAMP(7)
    }
}

constexpr int LAG = 10;
constexpr int DGRP = 3;
constexpr int NT_I = 16 * 96;
constexpr int NT_G = 16 + 16 + 96 + 16 + 16;
constexpr int NT_ALL = NT_I + (16 + DGRP) * NT_G;

__global__ void __launch_bounds__(512, 2) k_mega(const u16* __restrict__ mem0, char* __restrict__ ws_, u16* __restrict__ out_) {
    __shared__ __attribute__((aligned(16))) char smem[LDS_BYTES];
    __shared__ int s_ticket;
    if (blockIdx.x < NCH) { chain_role(mem0, ws_, smem, LAG, NSEG_ + LAG); return; }
    for (;;) {
        __syncthreads();
        if (threadIdx.x == 0) s_ticket = (int)__hip_atomic_fetch_add(flag_ptr(ws_, FL_TICKET), 1u, __ATOMIC_RELAXED, __HIP_MEMORY_SCOPE_AGENT);
        __syncthreads();
        int tk = __builtin_amdgcn_readfirstlane(s_ticket);
        if (tk >= NT_ALL) break;
        char* ws = ws_; asm volatile("" : "+s"(ws));
#define TRC nullptr
        u16* out = out_; asm volatile("" : "+s"(out));
        int tid = threadIdx.x; asm volatile("" : "+v"(tid));
        if (tk < NT_I) {
            const int G = tk / 96, j = tk % 96, nb = (j + 32) % 96;
            TR(1) task_qkv(ws, smem, 0, G, nb, tid); TR(2)
            signal_flag(ws, FL_QKV + (0 * 16 + G) * 16 + ((nb >> 1) & 15));
            continue;
        }
        tk -= NT_I;
        const int G = tk / NT_G; int j = tk % NT_G;
        if (j < 16) {
            if (G < 16) {
                wait_flag(ws, FL_QKV + (0 * 16 + G) * 16 + j, 6u);
                wait_flag(ws, FL_MEM + (0 * 16 + G) * 16 + j, 16u);
                TR(1) task_att(ws, smem, 0, G, j, tid, TRC); TR(2)
                signal_flag(ws, FL_ATT + 0 * 16 + G);
            }
            continue;
        }
        j -= 16;
        if (j < 16) {
            if (G < 16) {
                wait_flag(ws, FL_ATT + 0 * 16 + G, 16u);
                TR(1) task_red(ws, out, 0, G * 16 + j, tid); TR(2)
                signal_flag(ws, FL_RED + 0 * 16 + G);
            }
            continue;
        }
        j -= 16;
        if (j < 96) {
            if (G < 16) {
                const int nb = (j + 32) % 96;
                wait_flag(ws, FL_RED + 0 * 16 + G, 16u);
                TR(1) task_qkv(ws, smem, 1, G, nb, tid); TR(2)
                signal_flag(ws, FL_QKV + (1 * 16 + G) * 16 + ((nb >> 1) & 15));
            }
            continue;
        }
        j -= 96;
        const int G1 = G - DGRP;
        if (G1 < 0) continue;
        if (j < 16) {
            wait_flag(ws, FL_QKV + (1 * 16 + G1) * 16 + j, 6u);
            wait_flag(ws, FL_MEM + (1 * 16 + G1) * 16 + j, 16u);
            TR(1) task_att(ws, smem, 1, G1, j, tid, TRC); TR(2)
            signal_flag(ws, FL_ATT + 1 * 16 + G1);
            continue;
        }
        j -= 16;
        wait_flag(ws, FL_ATT + 1 * 16 + G1, 16u);
        TR(1) task_red(ws, out, 1, G1 * 16 + j, tid); TR(2)
    }
}

extern "C" void kernel_launch(void* const* d_in, const int* in_sizes, int n_in, void* d_out, int out_size, void* d_ws, size_t ws_size, hipStream_t stream) {
    const u16* A_q = (const u16*)d_in[0];
    const u16* A_k = (const u16*)d_in[1];
    const u16* A_v = (const u16*)d_in[2];
    const u16* A_shk = (const u16*)d_in[3];
    const u16* A_ssmk = (const u16*)d_in[4];
    const u16* A_ssmD = (const u16*)d_in[5];
    const u16* B_mem0 = (const u16*)d_in[7];
    const u16* B_Wq = (const u16*)d_in[8];
    const u16* B_Wk = (const u16*)d_in[9];
    const u16* B_Wv = (const u16*)d_in[10];
    const u16* B_Wo = (const u16*)d_in[11];
    char* ws = (char*)d_ws;
    u16* out = (u16*)d_out;

    (void)hipMemsetAsync(ws + OFF_XA, 0xFF, OFF_FL - OFF_XA, stream);
    (void)hipMemsetAsync(ws + OFF_FL, 0, SZ_FL, stream);
    k_prep<<<3 * 448 + 448 + 224, 256, 0, stream>>>(B_Wq, B_Wk, B_Wv, B_Wo, A_k, A_v, A_shk, ws);
    k_conv<<<448, 256, 0, stream>>>(A_ssmk, A_ssmD, ws);
    k_stage1<<<64, 256, 0, stream>>>(A_q, ws);
    k_mega<<<256, 512, 0, stream>>>(B_mem0, ws, out);
}
```

```cpp
#include <hip/hip_runtime.h>
#include <hip/hip_bf16.h>
#include <stdint.h>

constexpr int S_ = 2048, D_ = 448, H_ = 16, DH_ = 256, NH_ = 4096, SEG_ = 32, NSEG_ = 64, MM_ = 8;

typedef unsigned short u16;
typedef __attribute__((ext_vector_type(8))) short bf16x8;
typedef __attribute__((ext_vector_type(4))) float f32x4;
typedef __attribute__((ext_vector_type(16))) float f32x16;
typedef unsigned v4u __attribute__((ext_vector_type(4)));
typedef unsigned v2u __attribute__((ext_vector_type(2)));

#define DI __device__ __forceinline__
#define MFMA32(a, b, c) __builtin_amdgcn_mfma_f32_32x32x16_bf16((a), (b), (c), 0, 0, 0)
#define MFMA16(a, b, c) __builtin_amdgcn_mfma_f32_16x16x32_bf16((a), (b), (c), 0, 0, 0)

DI float bf2f(u16 v) { return __uint_as_float(((unsigned)v) << 16); }
typedef __attribute__((ext_vector_type(2))) __bf16 bf16x2_t;
typedef __attribute__((ext_vector_type(2))) float f32x2;
DI unsigned pack2(float a, float b) { f32x2 f = {a, b}; return __builtin_bit_cast(unsigned, __builtin_convertvector(f, bf16x2_t)); }
DI u16 f2bf(float f) { return (u16)(pack2(f, 0.f) & 0xffffu); }
DI float rb(float f) { return __uint_as_float(pack2(f, 0.f) << 16); }

constexpr size_t al(size_t x) { return (x + 255) & ~(size_t)255; }
constexpr size_t OFF_WQKVT = 0;
constexpr size_t OFF_WOT   = OFF_WQKVT + al((size_t)12288 * 448 * 2);
constexpr size_t OFF_WOF   = OFF_WOT + al((size_t)448 * 4096 * 2);
constexpr size_t OFF_KVT   = OFF_WOF + al((size_t)448 * 4096 * 2);
constexpr size_t OFF_YT    = OFF_KVT + al((size_t)448 * 2048 * 2);
constexpr size_t OFF_Y     = OFF_YT + al((size_t)448 * 2048 * 2);
constexpr size_t OFF_X1    = OFF_Y + al((size_t)448 * 2048 * 2);
constexpr size_t OFF_X3    = OFF_X1 + al((size_t)2048 * 448 * 2);
constexpr size_t OFF_HB    = OFF_X3 + al((size_t)2048 * 448 * 2);
constexpr size_t OFF_Q     = OFF_HB + al((size_t)2 * 2048 * 448 * 2);
constexpr size_t OFF_K     = OFF_Q + al((size_t)2 * 2048 * 4096 * 2);
constexpr size_t OFF_VT    = OFF_K + al((size_t)2 * 2048 * 4096 * 2);
constexpr size_t OFF_KMEM  = OFF_VT + al((size_t)2 * 2048 * 4096 * 2);
constexpr size_t OFF_VMEMT = OFF_KMEM + al((size_t)2 * 64 * 8 * 4096 * 2);
constexpr size_t OFF_PART  = OFF_VMEMT + al((size_t)2 * 64 * 8 * 4096 * 2);
constexpr int RING = 4, NCH = 64, GG = 4;
constexpr int XA_UNITS = 320 + 128;
constexpr size_t OFF_XA    = OFF_PART + al((size_t)16 * 2048 * 448 * 4);
constexpr size_t SZ_XA     = (size_t)RING * NCH * GG * XA_UNITS * 16;
constexpr size_t OFF_XB    = OFF_XA + al(SZ_XA);
constexpr size_t SZ_XB     = (size_t)RING * NCH * 16 * 32 * 16;
constexpr size_t OFF_XC    = OFF_XB + al(SZ_XB);
constexpr size_t SZ_XC     = (size_t)RING * NCH * 896 * 16;
constexpr size_t OFF_FL    = OFF_XC + al(SZ_XC);
constexpr int FL_TICKET = 0, FL_ERR = 1, FL_QKV = 16, FL_MEM = FL_QKV + 512, FL_ATT = FL_MEM + 512, FL_RED = FL_ATT + 32, FL_N = FL_RED + 32;
constexpr size_t SZ_FL     = (size_t)FL_N * 64;
constexpr size_t WS_END    = OFF_FL + al(SZ_FL);

__device__ void transpose_tile(const u16* __restrict__ src, int C, int r0, int c0, u16* __restrict__ dst, int ldd, int dr0, u16 (*tile)[66]) {
    const int tid = threadIdx.x;
    for (int i = tid; i < 64 * 8; i += 256) {
        int r = i >> 3, cq = i & 7;
        v4u v = *(const v4u*)(src + (size_t)(r0 + r) * C + c0 + cq * 8);
        unsigned w[4] = {v.x, v.y, v.z, v.w};
#pragma unroll
        for (int j = 0; j < 4; ++j) { tile[r][cq * 8 + 2 * j] = (u16)(w[j] & 0xffff); tile[r][cq * 8 + 2 * j + 1] = (u16)(w[j] >> 16); }
    }
    __syncthreads();
    for (int i = tid; i < 64 * 8; i += 256) {
        int c = i >> 3, rq = i & 7;
        unsigned w[4];
#pragma unroll
        for (int j = 0; j < 4; ++j) w[j] = (unsigned)tile[rq * 8 + 2 * j][c] | ((unsigned)tile[rq * 8 + 2 * j + 1][c] << 16);
        v4u v = {w[0], w[1], w[2], w[3]};
        *(v4u*)(dst + (size_t)(dr0 + c0 + c) * ldd + r0 + rq * 8) = v;
    }
    __syncthreads();
}

__global__ void __launch_bounds__(256) k_prep(const u16* __restrict__ Wq, const u16* __restrict__ Wk, const u16* __restrict__ Wv, const u16* __restrict__ Wo,
                                               const u16* __restrict__ kin, const u16* __restrict__ vin, const u16* __restrict__ shk, char* __restrict__ ws) {
    __shared__ u16 tile[64][66];
    int b = blockIdx.x;
    if (b < 3 * 448) {
        int w = b / 448, t = b % 448;
        const u16* src = w == 0 ? Wq : (w == 1 ? Wk : Wv);
        int r0 = (t / 64) * 64, c0 = (t % 64) * 64;
        transpose_tile(src, 4096, r0, c0, (u16*)(ws + OFF_WQKVT), 448, w * 4096, tile);
        return;
    }
    b -= 3 * 448;
    if (b < 448) {
        int r0 = (b / 7) * 64, c0 = (b % 7) * 64;
        transpose_tile(Wo, 448, r0, c0, (u16*)(ws + OFF_WOT), 4096, 0, tile);
        u16* wof = (u16*)(ws + OFF_WOF);
        for (int i = threadIdx.x; i < 64 * 8; i += 256) {
            int dl = i & 63, eq = i >> 6;
            int e0 = r0 + eq * 8, d = c0 + dl;
            int h = e0 >> 8, ee = e0 & 255, ks = ee >> 4, hh = (ee >> 3) & 1, dt = d >> 5, r = d & 31;
            unsigned w[4];
#pragma unroll
            for (int j = 0; j < 4; ++j) w[j] = (unsigned)tile[eq * 8 + 2 * j][dl] | ((unsigned)tile[eq * 8 + 2 * j + 1][dl] << 16);
            v4u v = {w[0], w[1], w[2], w[3]};
            *(v4u*)(wof + ((((size_t)h * 14 + dt) * 16 + ks) * 64 + hh * 32 + r) * 8) = v;
        }
        return;
    }
    b -= 448;
    {
        int t0 = (b / 7) * 64, d0 = (b % 7) * 64;
        const int tid = threadIdx.x;
        for (int i = tid; i < 64 * 64; i += 256) {
            int tt = i >> 6, dd = i & 63;
            int t = t0 + tt, d = d0 + dd;
            float acc = 0.f;
#pragma unroll
            for (int j = 0; j < 4; ++j) {
                int ts = t + j - 3;
                float kv_ = ts >= 0 ? bf2f(kin[(size_t)ts * D_ + d]) : 0.f;
                float pr = rb(kv_ * bf2f(shk[d * 4 + (3 - j)]));
                acc = rb(acc + pr);
            }
            float kvv = rb(acc * bf2f(vin[(size_t)t * D_ + d]));
            tile[dd][tt] = f2bf(kvv);
        }
        __syncthreads();
        u16* kvT = (u16*)(ws + OFF_KVT);
        for (int i = tid; i < 64 * 32; i += 256) {
            int dd = i >> 5, tp = i & 31;
            unsigned w = (unsigned)tile[dd][2 * tp] | ((unsigned)tile[dd][2 * tp + 1] << 16);
            *(unsigned*)(kvT + (size_t)(d0 + dd) * S_ + t0 + 2 * tp) = w;
        }
    }
}

constexpr int CV_GS = 2088;
__global__ void __launch_bounds__(256) k_conv(const u16* __restrict__ ssmk, const u16* __restrict__ ssmD, char* __restrict__ ws) {
    __shared__ __attribute__((aligned(16))) u16 gcp[8 * CV_GS];
    __shared__ __attribute__((aligned(16))) u16 kvs[96 * 40];
    __shared__ float ysum[2048];
    const int d = blockIdx.x, tid = threadIdx.x, lane = tid & 63, wave = tid >> 6;
    const u16* hrow = ssmk + (size_t)d * S_;
    const u16* kvT = (const u16*)(ws + OFF_KVT) + (size_t)d * S_;
    for (int i = tid; i < 8 * CV_GS; i += 256) {
        int p = i / CV_GS, x = i % CV_GS;
        int m = x + p;
        u16 v = 0;
        if (m <= 2047) v = hrow[2047 - m];
        gcp[i] = v;
    }
    for (int i = tid; i < 96 * 40; i += 256) {
        int blk = i / 40, j = i % 40;
        u16 v = 0;
        if (blk >= 32 && j < 32) v = kvT[(blk - 32) * 32 + j];
        kvs[i] = v;
    }
    for (int i = tid; i < 2048; i += 256) ysum[i] = 0.f;
    __syncthreads();
    f32x16 acc0, acc1;
#pragma unroll
    for (int i = 0; i < 16; ++i) { acc0[i] = 0.f; acc1[i] = 0.f; }
    const int r = lane & 31, hh = lane >> 5;
    for (int dl = wave; dl < 64; dl += 4) {
#pragma unroll
        for (int ks = 0; ks < 2; ++ks) {
            int a = 32 * dl + r - 16 * ks - 8 * hh;
            int base = 2047 - a;
            int p = base & 7, q = base >> 3;
            bf16x8 af = *(const bf16x8*)(gcp + p * CV_GS + q * 8);
            {
                int blk = 32 + (32 + r - dl);
                bf16x8 bfrag = *(const bf16x8*)(kvs + blk * 40 + 16 * ks + 8 * hh);
                acc1 = MFMA32(af, bfrag, acc1);
            }
            if (dl < 32) {
                int blk = 32 + (r - dl);
                bf16x8 bfrag = *(const bf16x8*)(kvs + blk * 40 + 16 * ks + 8 * hh);
                acc0 = MFMA32(af, bfrag, acc0);
            }
        }
    }
#pragma unroll
    for (int reg = 0; reg < 16; ++reg) {
        int j = (reg & 3) + 8 * (reg >> 2) + 4 * hh;
        atomicAdd(&ysum[32 * r + j], acc0[reg]);
        atomicAdd(&ysum[32 * (32 + r) + j], acc1[reg]);
    }
    __syncthreads();
    const float Dd = bf2f(ssmD[d]);
    u16* yT = (u16*)(ws + OFF_YT) + (size_t)d * S_;
    for (int t = tid; t < 2048; t += 256) {
        float kvv = bf2f(kvs[(32 + (t >> 5)) * 40 + (t & 31)]);
        float y = rb(rb(ysum[t]) + rb(kvv * Dd));
        yT[t] = f2bf(y);
    }
}

DI float wave_sum(float v) {
#pragma unroll
    for (int m = 32; m >= 1; m >>= 1) v += __shfl_xor(v, m);
    return v;
}
DI void layernorm7(const float (&x)[7], float (&o)[7]) {
    float s = 0.f;
#pragma unroll
    for (int i = 0; i < 7; ++i) s += x[i];
    s = wave_sum(s);
    const float mean = s * (1.0f / 448.0f);
    float q = 0.f;
#pragma unroll
    for (int i = 0; i < 7; ++i) { float dlt = x[i] - mean; q += dlt * dlt; }
    q = wave_sum(q);
    const float m = rb(mean), v = rb(q * (1.0f / 448.0f));
    const float den = rb(sqrtf(rb(v + 1e-5f)));
#pragma unroll
    for (int i = 0; i < 7; ++i) o[i] = rb(rb(x[i] - m) / den);
}

__global__ void __launch_bounds__(256) k_stage1(const u16* __restrict__ x0, char* __restrict__ ws) {
    __shared__ u16 yt[32][450];
    const int t0 = blockIdx.x * 32, tid = threadIdx.x, lane = tid & 63, wave = tid >> 6;
    const u16* yT = (const u16*)(ws + OFF_YT);
    for (int i = tid; i < 448 * 16; i += 256) {
        int d = i >> 4, tp = i & 15;
        unsigned w = *(const unsigned*)(yT + (size_t)d * S_ + t0 + 2 * tp);
        yt[2 * tp][d] = (u16)(w & 0xffff); yt[2 * tp + 1][d] = (u16)(w >> 16);
    }
    __syncthreads();
    u16* x1 = (u16*)(ws + OFF_X1); u16* hb = (u16*)(ws + OFF_HB); u16* yr = (u16*)(ws + OFF_Y);
    for (int rr = wave; rr < 32; rr += 4) {
        int t = t0 + rr;
        float x[7], ln[7];
#pragma unroll
        for (int i = 0; i < 7; ++i) x[i] = bf2f(x0[(size_t)t * D_ + lane + 64 * i]);
        layernorm7(x, ln);
#pragma unroll
        for (int i = 0; i < 7; ++i) x[i] = rb(x[i] + rb(ln[i] * bf2f(yt[rr][lane + 64 * i])));
        layernorm7(x, ln);
#pragma unroll
        for (int i = 0; i < 7; ++i) {
            x1[(size_t)t * D_ + lane + 64 * i] = f2bf(x[i]); hb[(size_t)t * D_ + lane + 64 * i] = f2bf(ln[i]);
            yr[(size_t)t * D_ + lane + 64 * i] = yt[rr][lane + 64 * i];
        }
    }
}

constexpr unsigned SENT = 0xFFFFFFFFu;
DI bool okv(v4u v) { return v.x != SENT && v.y != SENT && v.z != SENT && v.w != SENT; }
DI unsigned san(unsigned w) { return w == SENT ? 0x7FC07FC0u : w; }
DI v4u san4(v4u v) { v4u o = {san(v.x), san(v.y), san(v.z), san(v.w)}; return o; }
constexpr int SPIN_CAP = 1 << 16;
constexpr int WAIT_CAP = 1 << 16;
constexpr int MS = 456;
constexpr int SSR = 44;
constexpr int GS = 72;
constexpr int OS = 264;
constexpr int LDS_BYTES = 80 * 1024;

DI float xmax16(float v) { auto r = __builtin_amdgcn_permlane16_swap(__float_as_uint(v), __float_as_uint(v), false, false); return fmaxf(__uint_as_float(r[0]), __uint_as_float(r[1])); }
DI float xmax32(float v) { auto r = __builtin_amdgcn_permlane32_swap(__float_as_uint(v), __float_as_uint(v), false, false); return fmaxf(__uint_as_float(r[0]), __uint_as_float(r[1])); }
DI float xsum16(float v) { auto r = __builtin_amdgcn_permlane16_swap(__float_as_uint(v), __float_as_uint(v), false, false); return __uint_as_float(r[0]) + __uint_as_float(r[1]); }
DI float xsum32(float v) { auto r = __builtin_amdgcn_permlane32_swap(__float_as_uint(v), __float_as_uint(v), false, false); return __uint_as_float(r[0]) + __uint_as_float(r[1]); }
template <int CTRL> DI float dpp_f(float v) { return __uint_as_float(__builtin_amdgcn_update_dpp(0u, __float_as_uint(v), CTRL, 0xF, 0xF, false)); }
DI float rowsum16(float v) {
    v += dpp_f<0x128>(v);
    v += dpp_f<0x124>(v);
    v += dpp_f<0x122>(v);
    v += dpp_f<0x121>(v);
    return v;
}

DI float rowmax16(float v) {
    v = fmaxf(v, dpp_f<0x128>(v));
    v = fmaxf(v, dpp_f<0x124>(v));
    v = fmaxf(v, dpp_f<0x122>(v));
    v = fmaxf(v, dpp_f<0x121>(v));
    return v;
}
#define TR(i)
#define STAMP(i)

#define PSTAG 12
#define POLL2(N, v, LDSTMT, ERRCODE) { \
    v4u pa_[N], pb_[N]; \
    { v4u (&d_)[N] = pa_; LDSTMT } \
    __builtin_amdgcn_s_sleep(PSTAG); \
    int sp_ = 0; \
    for (;;) { \
        asm volatile("" ::: "memory"); \
        { v4u (&d_)[N] = pb_; LDSTMT } \
        asm volatile("" ::: "memory"); \
        { bool ok_ = true; _Pragma("unroll") for (int i_ = 0; i_ < N; ++i_) ok_ = ok_ && okv(pa_[i_]); \
          if (__all(ok_)) { _Pragma("unroll") for (int i_ = 0; i_ < N; ++i_) v[i_] = pa_[i_]; break; } } \
        asm volatile("" ::: "memory"); \
        { v4u (&d_)[N] = pa_; LDSTMT } \
        asm volatile("" ::: "memory"); \
        { bool ok_ = true; _Pragma("unroll") for (int i_ = 0; i_ < N; ++i_) ok_ = ok_ && okv(pb_[i_]); \
          if (__all(ok_)) { _Pragma("unroll") for (int i_ = 0; i_ < N; ++i_) v[i_] = pb_[i_]; break; } } \
        if (++sp_ >= SPIN_CAP) { __hip_atomic_store(errw, ERRCODE, __ATOMIC_RELAXED, __HIP_MEMORY_SCOPE_AGENT); _Pragma("unroll") for (int i_ = 0; i_ < N; ++i_) v[i_] = pa_[i_]; break; } \
    } }

DI unsigned* flag_ptr(char* ws, int idx) { return (unsigned*)(ws + OFF_FL + (size_t)idx * 64); }
DI unsigned flag_load(char* ws, int idx) { return __hip_atomic_load(flag_ptr(ws, idx), __ATOMIC_RELAXED, __HIP_MEMORY_SCOPE_AGENT); }
DI void flag_add(char* ws, int idx) { __hip_atomic_fetch_add(flag_ptr(ws, idx), 1u, __ATOMIC_RELAXED, __HIP_MEMORY_SCOPE_AGENT); }
DI void wait_flag(char* ws, int idx, unsigned target) {
    if (threadIdx.x == 0) {
        int sp = 0;
        for (; sp < WAIT_CAP; ++sp) {
            if (flag_load(ws, idx) >= target) break;
            if (flag_load(ws, FL_ERR) != 0u) break;
            __builtin_amdgcn_s_sleep(8);
        }
        if (sp == WAIT_CAP) __hip_atomic_store(flag_ptr(ws, FL_ERR), 5u, __ATOMIC_RELAXED, __HIP_MEMORY_SCOPE_AGENT);
    }
    __syncthreads();
}
DI void signal_flag(char* ws, int idx) {
    __builtin_amdgcn_s_waitcnt(0x0F70);
    __syncthreads();
    if (threadIdx.x == 0) flag_add(ws, idx);
}

DI void task_qkv(char* ws, char* smem, int layer, int G, int nb, int tid) {
    u16* sW = (u16*)smem;
    u16* sH = sW + 2 * 128 * GS;
    const int lane = tid & 63, wave = tid >> 6;
    const int n0 = nb * 128, t0 = G * 128;
    const u16* WT = (const u16*)(ws + OFF_WQKVT) + (size_t)n0 * D_;
    auto rH = __builtin_amdgcn_make_buffer_rsrc(ws + OFF_HB + ((size_t)layer * S_ + t0) * D_ * 2, 0, 128 * D_ * 2, 0x00020000);
    const bool isV = nb >= 64;
    const int wr = wave >> 1, wc = wave & 1;
    const int r = lane & 31, hh = lane >> 5;
    f32x16 acc[2];
#pragma unroll
    for (int b = 0; b < 2; ++b)
#pragma unroll
        for (int i = 0; i < 16; ++i) acc[b][i] = 0.f;
    v4u pw[7][2], ph[7][2];
#pragma unroll
    for (int kb = 0; kb < 7; ++kb)
#pragma unroll
        for (int i = 0; i < 2; ++i) {
            int c = tid + 512 * i, row = c >> 3, kq = c & 7;
            ph[kb][i] = __builtin_amdgcn_raw_buffer_load_b128(rH, (row * D_ + kb * 64 + kq * 8) * 2, 0, 16);
            pw[kb][i] = *(const v4u*)(WT + (size_t)row * D_ + kb * 64 + kq * 8);
        }
#pragma unroll
    for (int i = 0; i < 2; ++i) { int c = tid + 512 * i, row = c >> 3, kq = c & 7; *(v4u*)(sW + row * GS + kq * 8) = pw[0][i]; *(v4u*)(sH + row * GS + kq * 8) = ph[0][i]; }
    __syncthreads();
#pragma unroll
    for (int kb = 0; kb < 7; ++kb) {
        const int cur = (kb & 1) * 128 * GS, nxt = ((kb + 1) & 1) * 128 * GS;
        if (kb < 6) {
#pragma unroll
            for (int i = 0; i < 2; ++i) { int c = tid + 512 * i, row = c >> 3, kq = c & 7; *(v4u*)(sW + nxt + row * GS + kq * 8) = pw[kb + 1][i]; *(v4u*)(sH + nxt + row * GS + kq * 8) = ph[kb + 1][i]; }
        }
#pragma unroll
        for (int ks = 0; ks < 4; ++ks) {
            bf16x8 fw = *(const bf16x8*)(sW + cur + (wr * 32 + r) * GS + ks * 16 + hh * 8);
            bf16x8 fh[2];
#pragma unroll
            for (int b = 0; b < 2; ++b) fh[b] = *(const bf16x8*)(sH + cur + (wc * 64 + b * 32 + r) * GS + ks * 16 + hh * 8);
            if (!isV) {
#pragma unroll
                for (int b = 0; b < 2; ++b) acc[b] = MFMA32(fw, fh[b], acc[b]);
            } else {
#pragma unroll
                for (int b = 0; b < 2; ++b) acc[b] = MFMA32(fh[b], fw, acc[b]);
            }
        }
        __syncthreads();
    }
    u16* sT = (u16*)smem;
    if (!isV) {
#pragma unroll
        for (int b = 0; b < 2; ++b) {
            int t = wc * 64 + b * 32 + r;
#pragma unroll
            for (int g4 = 0; g4 < 4; ++g4) {
                int n = wr * 32 + 8 * g4 + 4 * hh;
                v2u v = {pack2(acc[b][4 * g4], acc[b][4 * g4 + 1]), pack2(acc[b][4 * g4 + 2], acc[b][4 * g4 + 3])};
                *(v2u*)(sT + t * 136 + n) = v;
            }
        }
        __syncthreads();
        auto rD = __builtin_amdgcn_make_buffer_rsrc(ws + (nb < 32 ? OFF_Q : OFF_K) + ((size_t)layer * S_ + t0) * NH_ * 2, 0, 128 * NH_ * 2, 0x00020000);
        const int hd = (nb >> 1) & 15, c0 = (nb & 1) * 16;
#pragma unroll
        for (int k = 0; k < 4; ++k) {
            int i = tid + 512 * k, sg = i >> 9, c = (i >> 5) & 15, row = i & 31;
            v4u v = *(const v4u*)(sT + (sg * 32 + row) * 136 + c * 8);
            __builtin_amdgcn_raw_buffer_store_b128(v, rD, (((sg * 16 + hd) * 32 + c0 + c) * 32 + row) * 16, 0, 16);
        }
    } else {
#pragma unroll
        for (int b = 0; b < 2; ++b) {
            int n = wr * 32 + r;
#pragma unroll
            for (int g4 = 0; g4 < 4; ++g4) {
                int t = wc * 64 + b * 32 + 8 * g4 + 4 * hh;
                v2u v = {pack2(acc[b][4 * g4], acc[b][4 * g4 + 1]), pack2(acc[b][4 * g4 + 2], acc[b][4 * g4 + 3])};
                *(v2u*)(sT + n * 136 + t) = v;
            }
        }
        __syncthreads();
        auto rD = __builtin_amdgcn_make_buffer_rsrc(ws + OFF_VT + ((size_t)layer * S_ + t0) * NH_ * 2, 0, 128 * NH_ * 2, 0x00020000);
        const int hd = (nb >> 1) & 15, e0 = (nb & 1) * 128;
#pragma unroll
        for (int k = 0; k < 4; ++k) {
            int i = tid + 512 * k, sg = i >> 9, kc = (i >> 7) & 3, e = i & 127;
            v4u v = *(const v4u*)(sT + e * 136 + sg * 32 + kc * 8);
            __builtin_amdgcn_raw_buffer_store_b128(v, rD, (((sg * 16 + hd) * 4 + kc) * 256 + e0 + e) * 16, 0, 16);
        }
    }
}

DI void task_att(char* ws, char* smem, int layer, int G, int h, int tid, unsigned long long* trc) {
    u16* so = (u16*)smem;
    v4u* sP = (v4u*)(smem + 128 * OS * 2);
    const int lane = tid & 63, wave = tid >> 6;
    const int sg = wave & 3, role = wave >> 2;
    const int seg = G * 4 + sg;
    const int r = lane & 31, hh = lane >> 5;
    const int pr = (r & 19) | ((r & 4) << 1) | ((r & 8) >> 1);
    const size_t sh = ((size_t)layer * NSEG_ + seg) * 16 + h;
    const u16* WoF = (const u16*)(ws + OFF_WOF) + (size_t)h * 14 * 8192;
    const v4u zero4 = {0u, 0u, 0u, 0u};
    bf16x8 wf[16];
    v4u va[8][2], vm[8];
    if (role == 0) {
        auto rQ = __builtin_amdgcn_make_buffer_rsrc(ws + OFF_Q + sh * 8192 * 2, 0, 8192 * 2, 0x00020000);
        auto rK = __builtin_amdgcn_make_buffer_rsrc(ws + OFF_K + sh * 8192 * 2, 0, 8192 * 2, 0x00020000);
        auto rKM = __builtin_amdgcn_make_buffer_rsrc(ws + OFF_KMEM + sh * 2048 * 2, 0, 2048 * 2, 0x00020000);
        f32x16 s1, s0;
#pragma unroll
        for (int i = 0; i < 16; ++i) { s1[i] = 0.f; s0[i] = 0.f; }
        v4u qf[16], kf[16], mf[16];
#pragma unroll
        for (int ks = 0; ks < 16; ++ks) {
            qf[ks] = __builtin_amdgcn_raw_buffer_load_b128(rQ, ((2 * ks + hh) * 32 + r) * 16, 0, 16);
            kf[ks] = __builtin_amdgcn_raw_buffer_load_b128(rK, ((2 * ks + hh) * 32 + pr) * 16, 0, 16);
            mf[ks] = __builtin_amdgcn_raw_buffer_load_b128(rKM, ((2 * ks + hh) * 8 + (pr & 7)) * 16, 0, 16);
        }
#pragma unroll
        for (int ks = 0; ks < 16; ++ks) {
            s1 = MFMA32(__builtin_bit_cast(bf16x8, kf[ks]), __builtin_bit_cast(bf16x8, qf[ks]), s1);
            v4u m = pr < 8 ? mf[ks] : zero4;
            s0 = MFMA32(__builtin_bit_cast(bf16x8, m), __builtin_bit_cast(bf16x8, qf[ks]), s0);
        }
        TR(3)
        float sc[16], sm[8];
        float mx = -3.0e38f;
#pragma unroll
        for (int reg = 0; reg < 16; ++reg) {
            int key = 16 * (reg >> 3) + 8 * hh + (reg & 7);
            float v = rb(s1[reg]) * 0.0625f;
            v = (key <= r) ? v : -1000000000.0f;
            sc[reg] = v; mx = fmaxf(mx, v);
        }
#pragma unroll
        for (int reg = 0; reg < 8; ++reg) { float v = rb(s0[reg]) * 0.0625f; sm[reg] = v; if (hh == 0) mx = fmaxf(mx, v); }
        mx = xmax32(mx);
        float sum = 0.f;
#pragma unroll
        for (int reg = 0; reg < 16; ++reg) { sc[reg] = __expf(sc[reg] - mx); sum += sc[reg]; }
#pragma unroll
        for (int reg = 0; reg < 8; ++reg) { sm[reg] = hh == 0 ? __expf(sm[reg] - mx) : 0.f; sum += sm[reg]; }
        sum = xsum32(sum);
        const float inv = 1.0f / sum;
#pragma unroll
        for (int s = 0; s < 2; ++s) {
            v4u w = {pack2(sc[8 * s] * inv, sc[8 * s + 1] * inv), pack2(sc[8 * s + 2] * inv, sc[8 * s + 3] * inv), pack2(sc[8 * s + 4] * inv, sc[8 * s + 5] * inv), pack2(sc[8 * s + 6] * inv, sc[8 * s + 7] * inv)};
            sP[(sg * 3 + s) * 64 + lane] = w;
        }
        { v4u w = {pack2(sm[0] * inv, sm[1] * inv), pack2(sm[2] * inv, sm[3] * inv), pack2(sm[4] * inv, sm[5] * inv), pack2(sm[6] * inv, sm[7] * inv)}; sP[(sg * 3 + 2) * 64 + lane] = w; }
        TR(4)
    } else {
        auto rV = __builtin_amdgcn_make_buffer_rsrc(ws + OFF_VT + sh * 8192 * 2, 0, 8192 * 2, 0x00020000);
        auto rVM = __builtin_amdgcn_make_buffer_rsrc(ws + OFF_VMEMT + sh * 2048 * 2, 0, 2048 * 2, 0x00020000);
#pragma unroll
        for (int et = 0; et < 8; ++et) {
#pragma unroll
            for (int s = 0; s < 2; ++s) va[et][s] = __builtin_amdgcn_raw_buffer_load_b128(rV, ((2 * s + hh) * 256 + et * 32 + r) * 16, 0, 16);
            vm[et] = __builtin_amdgcn_raw_buffer_load_b128(rVM, (et * 32 + r) * 16, 0, 16);
        }
    }
#pragma unroll
    for (int ks = 0; ks < 16; ++ks) wf[ks] = *(const bf16x8*)(WoF + ((size_t)(wave * 16 + ks) * 64 + lane) * 8);
    __syncthreads();
    if (role == 1) {
        bf16x8 pf[3];
#pragma unroll
        for (int s = 0; s < 3; ++s) pf[s] = __builtin_bit_cast(bf16x8, sP[(sg * 3 + s) * 64 + lane]);
#pragma unroll
        for (int et = 0; et < 8; ++et) {
            f32x16 o;
#pragma unroll
            for (int i = 0; i < 16; ++i) o[i] = 0.f;
#pragma unroll
            for (int s = 0; s < 2; ++s) o = MFMA32(__builtin_bit_cast(bf16x8, va[et][s]), pf[s], o);
            o = MFMA32(__builtin_bit_cast(bf16x8, hh == 0 ? vm[et] : zero4), pf[2], o);
#pragma unroll
            for (int g4 = 0; g4 < 4; ++g4) {
                v2u v = {pack2(o[4 * g4], o[4 * g4 + 1]), pack2(o[4 * g4 + 2], o[4 * g4 + 3])};
                *(v2u*)(so + (sg * 32 + r) * OS + et * 32 + 8 * g4 + 4 * hh) = v;
            }
        }
    }
    __syncthreads();
    TR(5)
    auto rP = __builtin_amdgcn_make_buffer_rsrc(ws + OFF_PART + ((size_t)h * S_ + G * 128) * D_ * 4, 0, 128 * D_ * 4, 0x00020000);
#pragma unroll
    for (int rnd = 0; rnd < 2; ++rnd) {
        const int dt = wave + 8 * rnd;
        if (dt < 14) {
            f32x16 acc[4];
#pragma unroll
            for (int b = 0; b < 4; ++b)
#pragma unroll
                for (int i = 0; i < 16; ++i) acc[b][i] = 0.f;
            bf16x8 wc[16];
#pragma unroll
            for (int ks = 0; ks < 16; ++ks) wc[ks] = wf[ks];
            if (rnd == 1) { TR(6) }
            if (rnd == 0 && wave + 8 < 14) {
#pragma unroll
                for (int ks = 0; ks < 16; ++ks) wf[ks] = *(const bf16x8*)(WoF + ((size_t)((wave + 8) * 16 + ks) * 64 + lane) * 8);
            }
#pragma unroll
            for (int ks = 0; ks < 16; ++ks) {
#pragma unroll
                for (int b = 0; b < 4; ++b) {
                    bf16x8 of = *(const bf16x8*)(so + (b * 32 + r) * OS + ks * 16 + hh * 8);
                    acc[b] = MFMA32(of, wc[ks], acc[b]);
                }
            }
#pragma unroll
            for (int b = 0; b < 4; ++b)
#pragma unroll
                for (int reg = 0; reg < 16; ++reg) {
                    int t = b * 32 + (reg & 3) + 8 * (reg >> 2) + 4 * hh;
                    __builtin_amdgcn_raw_buffer_store_b32(__float_as_uint(acc[b][reg]), rP, (t * D_ + dt * 32 + r) * 4, 0, 16);
                }
        }
    }
}

DI void layernorm8(const float (&x)[8], float (&o)[8], bool on) {
    float s = 0.f;
#pragma unroll
    for (int i = 0; i < 8; ++i) s += x[i];
    s = wave_sum(s);
    const float mean = s * (1.0f / 448.0f);
    float q = 0.f;
#pragma unroll
    for (int i = 0; i < 8; ++i) { float dlt = x[i] - mean; q += on ? dlt * dlt : 0.f; }
    q = wave_sum(q);
    const float m = rb(mean), v = rb(q * (1.0f / 448.0f));
    const float den = rb(sqrtf(rb(v + 1e-5f)));
#pragma unroll
    for (int i = 0; i < 8; ++i) o[i] = on ? rb(rb(x[i] - m) / den) : 0.f;
}
DI void unpack8(v4u w, float (&x)[8]) {
    x[0] = __uint_as_float(w.x << 16); x[1] = __uint_as_float(w.x & 0xffff0000u); x[2] = __uint_as_float(w.y << 16); x[3] = __uint_as_float(w.y & 0xffff0000u);
    x[4] = __uint_as_float(w.z << 16); x[5] = __uint_as_float(w.z & 0xffff0000u); x[6] = __uint_as_float(w.w << 16); x[7] = __uint_as_float(w.w & 0xffff0000u);
}
DI v4u pack8(const float (&x)[8]) { v4u w = {pack2(x[0], x[1]), pack2(x[2], x[3]), pack2(x[4], x[5]), pack2(x[6], x[7])}; return w; }
DI void task_red(char* ws, u16* __restrict__ out, int layer, int rb8, int tid) {
    const int lane = tid & 63, wave = tid >> 6;
    const bool on = lane < 56;
    const int lc = on ? lane : 0;
    auto rP = __builtin_amdgcn_make_buffer_rsrc(ws + OFF_PART, 0, (unsigned)((size_t)16 * S_ * D_ * 4), 0x00020000);
    auto rX = __builtin_amdgcn_make_buffer_rsrc(ws + (layer == 0 ? OFF_X1 : OFF_X3), 0, S_ * D_ * 2, 0x00020000);
    auto rX3 = __builtin_amdgcn_make_buffer_rsrc(ws + OFF_X3, 0, S_ * D_ * 2, 0x00020000);
    auto rH3 = __builtin_amdgcn_make_buffer_rsrc(ws + OFF_HB + (size_t)S_ * D_ * 2, 0, S_ * D_ * 2, 0x00020000);
    const u16* yr = (const u16*)(ws + OFF_Y);
    const int t = rb8 * 8 + wave;
    f32x4 pa[16], pb[16];
#pragma unroll
    for (int h = 0; h < 16; ++h) {
        pa[h] = __builtin_bit_cast(f32x4, __builtin_amdgcn_raw_buffer_load_b128(rP, ((h * S_ + t) * D_ + lc * 8) * 4, 0, 16));
        pb[h] = __builtin_bit_cast(f32x4, __builtin_amdgcn_raw_buffer_load_b128(rP, ((h * S_ + t) * D_ + lc * 8 + 4) * 4, 0, 16));
    }
    v4u xw = __builtin_amdgcn_raw_buffer_load_b128(rX, (t * D_ + lc * 8) * 2, 0, 16);
    v4u yw = *(const v4u*)(yr + (size_t)t * D_ + lc * 8);
    float x[8], ln[8], y[8];
    unpack8(xw, x); unpack8(yw, y);
#pragma unroll
    for (int i = 0; i < 4; ++i) {
        float sa = 0.f, sb = 0.f;
#pragma unroll
        for (int h = 0; h < 16; ++h) { sa += pa[h][i]; sb += pb[h][i]; }
        x[i] = on ? rb(x[i] + rb(sa)) : 0.f; x[4 + i] = on ? rb(x[4 + i] + rb(sb)) : 0.f;
    }
    if (layer == 1) {
        if (on) *(v4u*)(out + (size_t)t * D_ + lane * 8) = pack8(x);
    } else {
        layernorm8(x, ln, on);
#pragma unroll
        for (int i = 0; i < 8; ++i) x[i] = on ? rb(x[i] + rb(ln[i] * y[i])) : 0.f;
        layernorm8(x, ln, on);
        if (on) {
            __builtin_amdgcn_raw_buffer_store_b128(pack8(x), rX3, (t * D_ + lane * 8) * 2, 0, 16);
            __builtin_amdgcn_raw_buffer_store_b128(pack8(ln), rH3, (t * D_ + lane * 8) * 2, 0, 16);
        }
    }
}

#define BLD(r, vo, so) __builtin_amdgcn_raw_buffer_load_b128((r), (vo), (so), 16)
#define BST(v, r, vo, so) __builtin_amdgcn_raw_buffer_store_b128((v), (r), (vo), (so), 16)
#define BST64(v, r, vo, so) __builtin_amdgcn_raw_buffer_store_b64((v), (r), (vo), (so), 16)
DI void chain_role(const u16* __restrict__ mem0, char* __restrict__ ws, char* smem, int start1, int nticks) {
    u16* sMem = (u16*)smem;
    u16* sQ = sMem + 16 * MS;
    u16* sKm = sQ + 16 * 72;
    u16* sVT = sKm + 16 * 72;
    float* sS = (float*)(sVT + 256 * 24);
    u16* sO = (u16*)(sS + 16 * SSR);
    u16* sM = sO + 16 * 264;
    u16* sP = sM + 128;
    const int wg = blockIdx.x, tid = threadIdx.x, lane = tid & 63;
    const int wave = __builtin_amdgcn_readfirstlane(tid >> 6);
    const int h = wg >> 2, g = wg & 3;
    const int r16 = lane & 15, kg = lane >> 4;
    const int start0 = 0;
    const u16* WT = (const u16*)(ws + OFF_WQKVT);
    const u16* WoT = (const u16*)(ws + OFF_WOT);
    auto rX = __builtin_amdgcn_make_buffer_rsrc(ws + OFF_XA, 0, (unsigned)(OFF_FL - OFF_XA), 0x00020000);
    auto rD = __builtin_amdgcn_make_buffer_rsrc(ws + OFF_K, 0, (unsigned)(OFF_PART - OFF_K), 0x00020000);
    constexpr unsigned XB0 = (unsigned)(OFF_XB - OFF_XA), XC0 = (unsigned)(OFF_XC - OFF_XA);
    constexpr unsigned VT0 = (unsigned)(OFF_VT - OFF_K), KM0 = (unsigned)(OFF_KMEM - OFF_K), VM0 = (unsigned)(OFF_VMEMT - OFF_K);
    unsigned* errw = flag_ptr(ws, FL_ERR);
    const v4u sent4 = {SENT, SENT, SENT, SENT};
    const bf16x8 zero8 = {0, 0, 0, 0, 0, 0, 0, 0};
    const v4u zero4 = {0u, 0u, 0u, 0u};

    bf16x8 wreg[30];
    {
        const int w4 = wave & 3;
        const int m0 = wave < 4 ? 0 : 2;
#pragma unroll
        for (int ks = 0; ks < 14; ++ks)
            wreg[ks] = *(const bf16x8*)(WT + (size_t)(m0 * 4096 + h * DH_ + g * 64 + w4 * 16 + r16) * D_ + ks * 32 + kg * 8);
        if (wave < 4) {
#pragma unroll
            for (int ks = 0; ks < 14; ++ks)
                wreg[14 + ks] = *(const bf16x8*)(WT + (size_t)(1 * 4096 + h * DH_ + g * 64 + w4 * 16 + r16) * D_ + ks * 32 + kg * 8);
            wreg[28] = zero8; wreg[29] = zero8;
        } else {
#pragma unroll
            for (int tl = 0; tl < 2; ++tl)
#pragma unroll
                for (int ks = 0; ks < 8; ++ks) {
                    int dt = 2 * w4 + tl;
                    wreg[14 + tl * 8 + ks] = (dt < 7) ? *(const bf16x8*)(WoT + (size_t)(g * 112 + dt * 16 + r16) * NH_ + h * DH_ + ks * 32 + kg * 8) : zero8;
                }
        }
    }
    for (int i = tid; i < 16 * MS; i += 512) sMem[i] = 0;
    for (int i = tid; i < 256 * 24; i += 512) sVT[i] = 0;

    const int cr = r16 >> 3;
    const int ck = kg >> 1;
    const unsigned vT = (unsigned)tid * 16u;
    const int vu = tid - 320;
    const bool v3 = vu < 128;
    unsigned vKV, vAv = 0;
    if (wave < 4) vKV = KM0 + (unsigned)((((cr * NSEG_) * 16 * 32 + wave * 2 + (kg >> 1)) * 8 + (r16 & 7)) * 16 + (kg & 1) * 8);
    else {
        const int e = (wave - 4) * 16 + r16;
        vKV = VM0 + (unsigned)((((ck * NSEG_) * NH_ + e) * 8 + 4 * (kg & 1)) * 2);
        vAv = (unsigned)(320 * 16 + e * 32 + kg * 8);
    }
    unsigned vP0 = 0, vP1 = 0, vP2 = 0;
    if (wave >= 5) {
        auto f = [&](int x) { return (unsigned)(((x >> 7) * XA_UNITS + 320 + (x & 127)) * 16); };
        vP0 = f(vu); vP1 = f(vu + 192); vP2 = v3 ? f(vu + 384) : vP1;
    }
    const unsigned vBs = XB0 + (unsigned)(((r16 * GG) * 16 * 32 + kg) * 16);
    const unsigned vBp = XB0 + (unsigned)((r16 * 32 + wave * 4 + kg) * 16);
    const int pc0 = tid / 14, pk0 = tid % 14, pc1 = (tid + 512) / 14, pk1 = (tid + 512) % 14;
    const unsigned vPub0 = XC0 + (unsigned)((pc0 * 896 + pk0) * 16), vPub1 = XC0 + (unsigned)((pc1 * 896 + pk1) * 16);
    const bool pub1 = tid < NCH * 14 - 512;
    const int kxc = (wave - 1) >> 1;
    const unsigned vKX = (unsigned)((kg * 32 + ((wave - 1) & 1) * 16 + r16) * 16);
    const unsigned vVX = VT0 + (unsigned)((kg * 256 + wave * 32 + r16) * 16);
    const bool svalid = (wave == 0) ? ((kg >> 1) == cr) : (kxc == cr);

    bf16x8 kxf[2] = {zero8, zero8};
    bf16x8 vxf[2][2];
    auto load_x = [&](int T) {
        int t0 = T - start0, t1 = T - start1;
        t0 = t0 < 0 ? 0 : (t0 > NSEG_ - 1 ? NSEG_ - 1 : t0);
        t1 = t1 < 0 ? 0 : (t1 > NSEG_ - 1 ? NSEG_ - 1 : t1);
        const unsigned b0 = (unsigned)((0 * NSEG_ + t0) * 16 + h) * 16384u, b1 = (unsigned)((1 * NSEG_ + t1) * 16 + h) * 16384u;
        if (wave >= 1 && wave <= 4) {
            const unsigned so = (kxc ? b1 : b0) + (unsigned)(g * 8 * 32 * 16);
            kxf[0] = __builtin_bit_cast(bf16x8, BLD(rD, vKX, so));
            kxf[1] = __builtin_bit_cast(bf16x8, BLD(rD, vKX, so + 4 * 32 * 16));
        }
        vxf[0][0] = __builtin_bit_cast(bf16x8, BLD(rD, vVX, b0));
        vxf[0][1] = __builtin_bit_cast(bf16x8, BLD(rD, vVX, b0 + 256));
        vxf[1][0] = __builtin_bit_cast(bf16x8, BLD(rD, vVX, b1));
        vxf[1][1] = __builtin_bit_cast(bf16x8, BLD(rD, vVX, b1 + 256));
    };
    auto wait_qkv = [&](int T) {
#pragma unroll
        for (int c = 0; c < 2; ++c) {
            const int tc = T - (c ? start1 : start0);
            if (tc >= 0 && tc < NSEG_ && (tc & 3) == 0) {
                int sp = 0;
                for (; sp < WAIT_CAP; ++sp) {
                    if (flag_load(ws, FL_QKV + (c * 16 + (tc >> 2)) * 16 + h) >= 6u) break;
                    if (flag_load(ws, FL_ERR) != 0u) break;
                    __builtin_amdgcn_s_sleep(16);
                }
                if (sp == WAIT_CAP) __hip_atomic_store(errw, 6u, __ATOMIC_RELAXED, __HIP_MEMORY_SCOPE_AGENT);
            }
        }
    };
    wait_qkv(0);
    load_x(0);
    __syncthreads();

    for (int T = 0; T < nticks; ++T) {
        const int sl = T & (RING - 1), sn = (T + 1) & (RING - 1), spv = (T - 1) & (RING - 1);
        const int tc0 = T - start0, tc1 = T - start1;
        const bool act0 = tc0 >= 0 && tc0 < NSEG_;
        const bool act1 = tc1 >= 0 && tc1 < NSEG_;
        const unsigned sC = XC0 + (unsigned)((sl * NCH + wg) * 896 * 16);
        const bool doC = T > 0 && tid < 448;
        {
            v4u v0 = zero4, v1 = zero4;
            if (T > 0 && wave < 7) BST(sent4, rX, vBp, (unsigned)((spv * NCH + wg) * 16 * 32 * 16));
            asm volatile("" ::: "memory");
            if (doC) {
                int sp = 0;
                do {
                    asm volatile("" ::: "memory");
                    v0 = BLD(rX, vT, sC);
                    v1 = BLD(rX, vT, sC + 448 * 16);
                    if (++sp == SPIN_CAP) { __hip_atomic_store(errw, 1u, __ATOMIC_RELAXED, __HIP_MEMORY_SCOPE_AGENT); break; }
                } while (!(okv(v0) && okv(v1)));
            }
            if (tid < 448) {
                const int row = tid / 56, c = tid % 56;
                if (!act0) v0 = zero4;
                if (!act1) v1 = zero4;
                if ((act0 && tc0 == 0) || (act1 && tc1 == 0)) {
                    v4u m = *(const v4u*)(mem0 + (size_t)row * D_ + c * 8);
                    if (act0 && tc0 == 0) v0 = m;
                    if (act1 && tc1 == 0) v1 = m;
                }
                *(v4u*)(sMem + row * MS + c * 8) = v0;
                *(v4u*)(sMem + (8 + row) * MS + c * 8) = v1;
            }
        }
        __syncthreads();
        STAMP(0)
        const unsigned sA0 = (unsigned)(((sl * NCH + h * GG) * GG + g) * XA_UNITS * 16);
        v2u kvm;
        if (wave < 4) {
            f32x4 aq = {0.f, 0.f, 0.f, 0.f}, ak = {0.f, 0.f, 0.f, 0.f};
#pragma unroll
            for (int ks = 0; ks < 14; ++ks) {
                bf16x8 mf = *(const bf16x8*)(sMem + r16 * MS + ks * 32 + kg * 8);
                aq = MFMA16(wreg[ks], mf, aq);
                ak = MFMA16(wreg[14 + ks], mf, ak);
            }
            v2u vq = {pack2(aq[0], aq[1]), pack2(aq[2], aq[3])};
            v2u vk = {pack2(ak[0], ak[1]), pack2(ak[2], ak[3])};
            *(v2u*)(sQ + r16 * 72 + wave * 16 + 4 * kg) = vq;
            *(v2u*)(sKm + r16 * 72 + wave * 16 + 4 * kg) = vk;
            kvm = vk;
        } else {
            f32x4 av = {0.f, 0.f, 0.f, 0.f};
#pragma unroll
            for (int ks = 0; ks < 14; ++ks) {
                bf16x8 mf = *(const bf16x8*)(sMem + r16 * MS + ks * 32 + kg * 8);
                av = MFMA16(mf, wreg[ks], av);
            }
            v2u vv = {san(pack2(av[0], av[1])), san(pack2(av[2], av[3]))};
#pragma unroll
            for (int c = 0; c < GG; ++c) BST64(vv, rX, vAv, sA0 + c * GG * XA_UNITS * 16);
            kvm = vv;
        }
        __syncthreads();
        STAMP(1)
        if (wave < 5) {
            f32x4 s = {0.f, 0.f, 0.f, 0.f};
#pragma unroll
            for (int ks = 0; ks < 2; ++ks) {
                bf16x8 qf = *(const bf16x8*)(sQ + r16 * 72 + ks * 32 + kg * 8);
                bf16x8 kf = (kxc ? act1 : act0) ? kxf[ks] : zero8;
                if (wave == 0) { v4u kl = *(const v4u*)(sKm + r16 * 72 + ks * 32 + kg * 8); asm volatile("" : "+v"(kl.x), "+v"(kl.y), "+v"(kl.z), "+v"(kl.w)); kf = __builtin_bit_cast(bf16x8, kl); }
                s = MFMA16(kf, qf, s);
            }
            v4u v = san4(__builtin_bit_cast(v4u, s));
            if (svalid) {
#pragma unroll
                for (int c = 0; c < GG; ++c) BST(v, rX, vT, sA0 + c * GG * XA_UNITS * 16);
            }
        }
        asm volatile("" ::: "memory");
        {
            const unsigned t0o = (unsigned)(tc0 * 16 + h), t1o = (unsigned)(tc1 * 16 + h);
            if (wave < 4) {
                if (cr ? act1 : act0) BST64(kvm, rD, vKV + (cr ? t1o : t0o) * 4096u, (unsigned)(g * 8 * 8 * 16));
            } else {
                if (ck ? act1 : act0) BST64(kvm, rD, vKV + (ck ? tc1 : tc0) * (unsigned)(NH_ * 16), (unsigned)((h * DH_ + g * 64) * 16));
            }
        }
        if (doC) { BST(sent4, rX, vT, sC); BST(sent4, rX, vT, sC + 448 * 16); }
        const int n0 = T + 1 - start0, n1 = T + 1 - start1;
        const bool need0 = n0 >= 0 && n0 < NSEG_ && (n0 & 3) == 0, need1 = n1 >= 0 && n1 < NSEG_ && (n1 & 3) == 0;
        const unsigned fq0 = flag_load(ws, FL_QKV + (0 * 16 + ((n0 >> 2) & 15)) * 16 + h), fq1 = flag_load(ws, FL_QKV + (1 * 16 + ((n1 >> 2) & 15)) * 16 + h);
        STAMP(2)
        const unsigned sAp = (unsigned)((sl * NCH + wg) * GG * XA_UNITS * 16);
        if (wave < 5) {
            if (svalid) {
                v4u v[GG];
                int sp = 0;
                do {
                    asm volatile("" ::: "memory");
#pragma unroll
                    for (int p = 0; p < GG; ++p) v[p] = BLD(rX, vT, sAp + p * XA_UNITS * 16);
                    if (++sp == SPIN_CAP) { __hip_atomic_store(errw, 2u, __ATOMIC_RELAXED, __HIP_MEMORY_SCOPE_AGENT); break; }
                } while (!(okv(v[0]) && okv(v[1]) && okv(v[2]) && okv(v[3])));
                f32x4 s = __builtin_bit_cast(f32x4, v[0]);
#pragma unroll
                for (int p = 1; p < GG; ++p) s += __builtin_bit_cast(f32x4, v[p]);
                const int col = (wave == 0) ? (kg & 1) * 4 : 8 + ((wave - 1) & 1) * 16 + kg * 4;
                *(f32x4*)(sS + r16 * SSR + col) = s;
            }
        } else {
            v4u v0, v1, v2;
            int sp = 0;
            do {
                asm volatile("" ::: "memory");
                v0 = BLD(rX, vP0, sAp); v1 = BLD(rX, vP1, sAp); v2 = BLD(rX, vP2, sAp);
                if (++sp == SPIN_CAP) { __hip_atomic_store(errw, 3u, __ATOMIC_RELAXED, __HIP_MEMORY_SCOPE_AGENT); break; }
            } while (!(okv(v0) && okv(v1) && okv(v2)));
            { const int x = vu; *(v4u*)(sVT + (x >> 1) * 24 + (x & 1) * 8) = v0; }
            { const int x = vu + 192; *(v4u*)(sVT + (x >> 1) * 24 + (x & 1) * 8) = v1; }
            if (v3) { const int x = vu + 384; *(v4u*)(sVT + (x >> 1) * 24 + (x & 1) * 8) = v2; }
        }
        __syncthreads();
        STAMP(3)
        {
            const int row = 2 * wave + (lane >> 5), j = lane & 31;
            const float xr = sS[row * SSR + 8 + j];
            const float mr = sS[row * SSR + (j & 7)];
            const float x = rb(xr) * 0.0625f;
            const float m = j < 8 ? rb(mr) * 0.0625f : -3.0e38f;
            float mx = fmaxf(x, m);
            mx = rowmax16(mx); mx = xmax16(mx);
            const float ex = __expf(x - mx);
            const float em = j < 8 ? __expf(m - mx) : 0.f;
            float sum = ex + em;
            sum = rowsum16(sum); sum = xsum16(sum);
            const float inv = 1.0f / sum;
            sP[row * 48 + 8 + j] = f2bf(ex * inv);
            if (j < 8) sP[row * 48 + j] = f2bf(em * inv);
        }
        __syncthreads();
        {
            const bool hasm = kg == cr;
            const bf16x8 px = *(const bf16x8*)(sP + r16 * 48 + 8 + kg * 8);
            v4u pml = *(const v4u*)(sP + r16 * 48);
            asm volatile("" : "+v"(pml.x), "+v"(pml.y), "+v"(pml.z), "+v"(pml.w));
            const bf16x8 pm = hasm ? __builtin_bit_cast(bf16x8, pml) : zero8;
            const bf16x8 px0 = cr == 0 ? px : zero8, px1 = cr == 1 ? px : zero8;
#pragma unroll
            for (int tl = 0; tl < 2; ++tl) {
                const int et = wave * 2 + tl;
                bf16x8 vm = zero8;
                if (kg < 2) vm = *(const bf16x8*)(sVT + (et * 16 + r16) * 24 + kg * 8);
                f32x4 o = {0.f, 0.f, 0.f, 0.f};
                o = MFMA16(vm, pm, o);
                o = MFMA16(act0 ? vxf[0][tl] : zero8, px0, o);
                o = MFMA16(act1 ? vxf[1][tl] : zero8, px1, o);
                v2u v = {pack2(o[0], o[1]), pack2(o[2], o[3])};
                *(v2u*)(sO + r16 * 264 + et * 16 + 4 * kg) = v;
            }
        }
        __syncthreads();
        STAMP(4)
        const bool more = T + 1 < nticks;
        if (more && wave >= 4) {
            const int w4 = wave - 4;
            const unsigned sB = (unsigned)((((sl * NCH + g) * 16 + h) * 32 + w4 * 8) * 16);
#pragma unroll
            for (int tl = 0; tl < 2; ++tl) {
                const int dt = 2 * w4 + tl;
                if (dt < 7) {
                    f32x4 a = {0.f, 0.f, 0.f, 0.f};
#pragma unroll
                    for (int ks = 0; ks < 8; ++ks) {
                        bf16x8 of = *(const bf16x8*)(sO + r16 * 264 + ks * 32 + kg * 8);
                        a = MFMA16(wreg[14 + tl * 8 + ks], of, a);
                    }
                    v4u v = san4(__builtin_bit_cast(v4u, a));
                    BST(v, rX, vBs, sB + tl * 64);
                }
            }
        }
        asm volatile("" ::: "memory");
        if (wave < 5) {
            if (svalid) {
#pragma unroll
                for (int q = 0; q < GG; ++q) BST(sent4, rX, vT, sAp + q * XA_UNITS * 16);
            }
        } else {
            BST(sent4, rX, vP0, sAp); BST(sent4, rX, vP1, sAp);
            if (v3) BST(sent4, rX, vP2, sAp);
        }
        STAMP(5)
        {
            const unsigned sBp = (unsigned)((sl * NCH + wg) * 16 * 32 * 16);
            const bool doB = more && wave < 7;
            v4u v = zero4;
            asm volatile("" ::: "memory");
            if (doB) v = BLD(rX, vBp, sBp);
            asm volatile("" ::: "memory");
            if ((need0 && fq0 < 6u) || (need1 && fq1 < 6u)) wait_qkv(T + 1);
            load_x(T + 1);
            asm volatile("" ::: "memory");
            if (doB) {
                int sp = 0;
                while (!okv(v)) {
                    asm volatile("" ::: "memory");
                    v = BLD(rX, vBp, sBp);
                    if (++sp == SPIN_CAP) { __hip_atomic_store(errw, 4u, __ATOMIC_RELAXED, __HIP_MEMORY_SCOPE_AGENT); break; }
                }
                float s0 = rowsum16(__uint_as_float(v.x)), s1 = rowsum16(__uint_as_float(v.y)), s2 = rowsum16(__uint_as_float(v.z)), s3 = rowsum16(__uint_as_float(v.w));
                v2u o = {san(pack2(s0, s1)), san(pack2(s2, s3))};
                if (r16 == 0) *(v2u*)(sM + (wave * 4 + kg) * 4) = o;
            } else {
                __builtin_amdgcn_s_waitcnt(0x0F70);
            }
            __syncthreads();
            STAMP(6)
            if (more) {
                const unsigned sP = (unsigned)((sn * NCH * 896 + h * 56 + g * 14) * 16);
                BST(*(const v4u*)(sM + pk0 * 8), rX, vPub0, sP);
                if (pub1) BST(*(const v4u*)(sM + pk1 * 8), rX, vPub1, sP);
            }
            if (tid == 0) {
                if (act0) flag_add(ws, FL_MEM + (0 * 16 + (tc0 >> 2)) * 16 + h);
                if (act1) flag_add(ws, FL_MEM + (1 * 16 + (tc1 >> 2)) * 16 + h);
            }
        }
        STAMP(7)
    }
}

constexpr int LAG = 10;
constexpr int DGRP = 3;
constexpr int NT_I = 16 * 96;
constexpr int NT_G = 16 + 16 + 96 + 16 + 16;
constexpr int NT_ALL = NT_I + (16 + DGRP) * NT_G;

__global__ void __launch_bounds__(512, 2) k_mega(const u16* __restrict__ mem0, char* __restrict__ ws_, u16* __restrict__ out_) {
    __shared__ __attribute__((aligned(16))) char smem[LDS_BYTES];
    __shared__ int s_ticket;
    if (blockIdx.x < NCH) { chain_role(mem0, ws_, smem, LAG, NSEG_ + LAG); return; }
    for (;;) {
        __syncthreads();
        if (threadIdx.x == 0) s_ticket = (int)__hip_atomic_fetch_add(flag_ptr(ws_, FL_TICKET), 1u, __ATOMIC_RELAXED, __HIP_MEMORY_SCOPE_AGENT);
        __syncthreads();
        int tk = __builtin_amdgcn_readfirstlane(s_ticket);
        if (tk >= NT_ALL) break;
        char* ws = ws_; asm volatile("" : "+s"(ws));
#define TRC nullptr
        u16* out = out_; asm volatile("" : "+s"(out));
        int tid = threadIdx.x; asm volatile("" : "+v"(tid));
        if (tk < NT_I) {
            const int G = tk / 96, j = tk % 96, nb = (j + 32) % 96;
            TR(1) task_qkv(ws, smem, 0, G, nb, tid); TR(2)
            signal_flag(ws, FL_QKV + (0 * 16 + G) * 16 + ((nb >> 1) & 15));
            continue;
        }
        tk -= NT_I;
        const int G = tk / NT_G; int j = tk % NT_G;
        if (j < 16) {
            if (G < 16) {
                wait_flag(ws, FL_QKV + (0 * 16 + G) * 16 + j, 6u);
                wait_flag(ws, FL_MEM + (0 * 16 + G) * 16 + j, 16u);
                TR(1) task_att(ws, smem, 0, G, j, tid, TRC); TR(2)
                signal_flag(ws, FL_ATT + 0 * 16 + G);
            }
            continue;
        }
        j -= 16;
        if (j < 16) {
            if (G < 16) {
                wait_flag(ws, FL_ATT + 0 * 16 + G, 16u);
                TR(1) task_red(ws, out, 0, G * 16 + j, tid); TR(2)
                signal_flag(ws, FL_RED + 0 * 16 + G);
            }
            continue;
        }
        j -= 16;
        if (j < 96) {
            if (G < 16) {
                const int nb = (j + 32) % 96;
                wait_flag(ws, FL_RED + 0 * 16 + G, 16u);
                TR(1) task_qkv(ws, smem, 1, G, nb, tid); TR(2)
                signal_flag(ws, FL_QKV + (1 * 16 + G) * 16 + ((nb >> 1) & 15));
            }
            continue;
        }
        j -= 96;
        const int G1 = G - DGRP;
        if (G1 < 0) continue;
        if (j < 16) {
            wait_flag(ws, FL_QKV + (1 * 16 + G1) * 16 + j, 6u);
            wait_flag(ws, FL_MEM + (1 * 16 + G1) * 16 + j, 16u);
            TR(1) task_att(ws, smem, 1, G1, j, tid, TRC); TR(2)
            signal_flag(ws, FL_ATT + 1 * 16 + G1);
            continue;
        }
        j -= 16;
        wait_flag(ws, FL_ATT + 1 * 16 + G1, 16u);
        TR(1) task_red(ws, out, 1, G1 * 16 + j, tid); TR(2)
    }
}

extern "C" void kernel_launch(void* const* d_in, const int* in_sizes, int n_in, void* d_out, int out_size, void* d_ws, size_t ws_size, hipStream_t stream) {
    const u16* A_q = (const u16*)d_in[0];
    const u16* A_k = (const u16*)d_in[1];
    const u16* A_v = (const u16*)d_in[2];
    const u16* A_shk = (const u16*)d_in[3];
    const u16* A_ssmk = (const u16*)d_in[4];
    const u16* A_ssmD = (const u16*)d_in[5];
    const u16* B_mem0 = (const u16*)d_in[7];
    const u16* B_Wq = (const u16*)d_in[8];
    const u16* B_Wk = (const u16*)d_in[9];
    const u16* B_Wv = (const u16*)d_in[10];
    const u16* B_Wo = (const u16*)d_in[11];
    char* ws = (char*)d_ws;
    u16* out = (u16*)d_out;

    (void)hipMemsetAsync(ws + OFF_XA, 0xFF, OFF_FL - OFF_XA, stream);
    (void)hipMemsetAsync(ws + OFF_FL, 0, SZ_FL, stream);
    k_prep<<<3 * 448 + 448 + 224, 256, 0, stream>>>(B_Wq, B_Wk, B_Wv, B_Wo, A_k, A_v, A_shk, ws);
    k_conv<<<448, 256, 0, stream>>>(A_ssmk, A_ssmD, ws);
    k_stage1<<<64, 256, 0, stream>>>(A_q, ws);
    k_mega<<<256, 512, 0, stream>>>(B_mem0, ws, out);
}
```

```cpp
#include <hip/hip_runtime.h>
#include <hip/hip_bf16.h>
#include <stdint.h>

constexpr int S_ = 2048, D_ = 448, H_ = 16, DH_ = 256, NH_ = 4096, SEG_ = 32, NSEG_ = 64, MM_ = 8;

typedef unsigned short u16;
typedef __attribute__((ext_vector_type(8))) short bf16x8;
typedef __attribute__((ext_vector_type(4))) float f32x4;
typedef __attribute__((ext_vector_type(16))) float f32x16;
typedef unsigned v4u __attribute__((ext_vector_type(4)));
typedef unsigned v2u __attribute__((ext_vector_type(2)));

#define DI __device__ __forceinline__
#define MFMA32(a, b, c) __builtin_amdgcn_mfma_f32_32x32x16_bf16((a), (b), (c), 0, 0, 0)
#define MFMA16(a, b, c) __builtin_amdgcn_mfma_f32_16x16x32_bf16((a), (b), (c), 0, 0, 0)

DI float bf2f(u16 v) { return __uint_as_float(((unsigned)v) << 16); }
typedef __attribute__((ext_vector_type(2))) __bf16 bf16x2_t;
typedef __attribute__((ext_vector_type(2))) float f32x2;
DI unsigned pack2(float a, float b) { f32x2 f = {a, b}; return __builtin_bit_cast(unsigned, __builtin_convertvector(f, bf16x2_t)); }
DI u16 f2bf(float f) { return (u16)(pack2(f, 0.f) & 0xffffu); }
DI float rb(float f) { return __uint_as_float(pack2(f, 0.f) << 16); }

constexpr size_t al(size_t x) { return (x + 255) & ~(size_t)255; }
constexpr size_t OFF_WQKVT = 0;
constexpr size_t OFF_WOT   = OFF_WQKVT + al((size_t)12288 * 448 * 2);
constexpr size_t OFF_WOF   = OFF_WOT + al((size_t)448 * 4096 * 2);
constexpr size_t OFF_KVT   = OFF_WOF + al((size_t)448 * 4096 * 2);
constexpr size_t OFF_YT    = OFF_KVT + al((size_t)448 * 2048 * 2);
constexpr size_t OFF_Y     = OFF_YT + al((size_t)448 * 2048 * 2);
constexpr size_t OFF_X1    = OFF_Y + al((size_t)448 * 2048 * 2);
constexpr size_t OFF_X3    = OFF_X1 + al((size_t)2048 * 448 * 2);
constexpr size_t OFF_HB    = OFF_X3 + al((size_t)2048 * 448 * 2);
constexpr size_t OFF_Q     = OFF_HB + al((size_t)2 * 2048 * 448 * 2);
constexpr size_t OFF_K     = OFF_Q + al((size_t)2 * 2048 * 4096 * 2);
constexpr size_t OFF_VT    = OFF_K + al((size_t)2 * 2048 * 4096 * 2);
constexpr size_t OFF_KMEM  = OFF_VT + al((size_t)2 * 2048 * 4096 * 2);
constexpr size_t OFF_VMEMT = OFF_KMEM + al((size_t)2 * 64 * 8 * 4096 * 2);
constexpr size_t OFF_PART  = OFF_VMEMT + al((size_t)2 * 64 * 8 * 4096 * 2);
constexpr int RING = 4, NCH = 64, GG = 4;
constexpr int XA_UNITS = 320 + 128;
constexpr size_t OFF_XA    = OFF_PART + al((size_t)16 * 2048 * 448 * 4);
constexpr size_t SZ_XA     = (size_t)RING * NCH * GG * XA_UNITS * 16;
constexpr size_t OFF_XB    = OFF_XA + al(SZ_XA);
constexpr size_t SZ_XB     = (size_t)RING * NCH * 16 * 32 * 16;
constexpr size_t OFF_XC    = OFF_XB + al(SZ_XB);
constexpr size_t SZ_XC     = (size_t)RING * NCH * 896 * 16;
constexpr size_t OFF_FL    = OFF_XC + al(SZ_XC);
constexpr int FL_TICKET = 0, FL_ERR = 1, FL_QKV = 16, FL_MEM = FL_QKV + 512, FL_ATT = FL_MEM + 512, FL_RED = FL_ATT + 32, FL_N = FL_RED + 32;
constexpr size_t SZ_FL     = (size_t)FL_N * 64;
constexpr size_t WS_END    = OFF_FL + al(SZ_FL);

DI void tile_load(const u16* __restrict__ src, int C, int r0, int c0, unsigned (*T)[33]) {
    const int tid = threadIdx.x, rp = tid >> 3, cq = tid & 7;
    const v4u a = *(const v4u*)(src + (size_t)(r0 + 2 * rp) * C + c0 + cq * 8);
    const v4u b = *(const v4u*)(src + (size_t)(r0 + 2 * rp + 1) * C + c0 + cq * 8);
    const unsigned wa[4] = {a.x, a.y, a.z, a.w}, wb[4] = {b.x, b.y, b.z, b.w};
#pragma unroll
    for (int j = 0; j < 4; ++j) {
        T[cq * 8 + 2 * j][rp] = (wa[j] & 0xffffu) | (wb[j] << 16);
        T[cq * 8 + 2 * j + 1][rp] = (wa[j] >> 16) | (wb[j] & 0xffff0000u);
    }
}
DI void tile_store(unsigned (*T)[33], u16* __restrict__ dst, int ldd, int drow0, int dcol0) {
    const int tid = threadIdx.x;
#pragma unroll
    for (int it = 0; it < 2; ++it) {
        const int i = tid + 256 * it, c = i >> 3, rq = i & 7;
        v4u v = {T[c][rq * 4], T[c][rq * 4 + 1], T[c][rq * 4 + 2], T[c][rq * 4 + 3]};
        *(v4u*)(dst + (size_t)(drow0 + c) * ldd + dcol0 + rq * 8) = v;
    }
}

constexpr int HP = 2144;
constexpr int NB_KV = 224, NB_CONV = 448, NB_TR = 4 * 448;
constexpr size_t FILL_BYTES = OFF_FL - OFF_XA;
constexpr int NB_FILL = (int)((FILL_BYTES + 65535) / 65536);
constexpr int NB_ZERO = (int)((SZ_FL + 65535) / 65536);
constexpr int NB_PRE1 = NB_KV + NB_FILL + NB_ZERO;
constexpr int NB_PRE2 = NB_CONV + NB_TR;

__global__ void __launch_bounds__(256) k_pre1(const u16* __restrict__ kin, const u16* __restrict__ vin, const u16* __restrict__ shk, char* __restrict__ ws) {
    __shared__ unsigned T[64][33];
    int b = blockIdx.x;
    const int tid = threadIdx.x;
    if (b < NB_KV) {
        const int t0 = (b / 7) * 64, d0 = (b % 7) * 64;
        const int cq = tid & 7, tp = tid >> 3, t = t0 + 2 * tp, dc = d0 + 8 * cq;
        v4u kk[5], vv[2];
#pragma unroll
        for (int j = 0; j < 5; ++j) { const int ts = t - 3 + j; const v4u z = {0u, 0u, 0u, 0u}; kk[j] = ts >= 0 ? *(const v4u*)(kin + (size_t)ts * D_ + dc) : z; }
#pragma unroll
        for (int j = 0; j < 2; ++j) vv[j] = *(const v4u*)(vin + (size_t)(t + j) * D_ + dc);
        v4u sw4[4];
#pragma unroll
        for (int j = 0; j < 4; ++j) sw4[j] = *(const v4u*)(shk + dc * 4 + 8 * j);
#pragma unroll
        for (int c = 0; c < 8; ++c) {
            const unsigned sa = (c & 1) ? sw4[c >> 1][2] : sw4[c >> 1][0], sb = (c & 1) ? sw4[c >> 1][3] : sw4[c >> 1][1];
            const float sw[4] = {__uint_as_float(sa << 16), __uint_as_float(sa & 0xffff0000u), __uint_as_float(sb << 16), __uint_as_float(sb & 0xffff0000u)};
            float kc[5], vc[2];
#pragma unroll
            for (int j = 0; j < 5; ++j) { const unsigned w = kk[j][c >> 1]; kc[j] = __uint_as_float((c & 1) ? (w & 0xffff0000u) : (w << 16)); }
#pragma unroll
            for (int j = 0; j < 2; ++j) { const unsigned w = vv[j][c >> 1]; vc[j] = __uint_as_float((c & 1) ? (w & 0xffff0000u) : (w << 16)); }
            float kv[2];
#pragma unroll
            for (int j = 0; j < 2; ++j) {
                float acc = 0.f;
#pragma unroll
                for (int jj = 0; jj < 4; ++jj) { const float pr = rb(kc[j + jj] * sw[3 - jj]); acc = rb(acc + pr); }
                kv[j] = rb(acc * vc[j]);
            }
            T[8 * cq + c][(tp & 16) + 15 - (tp & 15)] = pack2(kv[1], kv[0]);
        }
        __syncthreads();
        tile_store(T, (u16*)(ws + OFF_KVT), S_, d0, t0);
        return;
    }
    b -= NB_KV;
    if (b < NB_FILL) {
        char* p = ws + OFF_XA + (size_t)b * 65536;
        const v4u s = {0xFFFFFFFFu, 0xFFFFFFFFu, 0xFFFFFFFFu, 0xFFFFFFFFu};
#pragma unroll
        for (int it = 0; it < 16; ++it) {
            const size_t o = (size_t)b * 65536 + (size_t)(it * 256 + tid) * 16;
            if (o < FILL_BYTES) *(v4u*)(p + (size_t)(it * 256 + tid) * 16) = s;
        }
        return;
    }
    b -= NB_FILL;
    {
        char* p = ws + OFF_FL + (size_t)b * 65536;
        const v4u s = {0u, 0u, 0u, 0u};
#pragma unroll
        for (int it = 0; it < 16; ++it) {
            const size_t o = (size_t)b * 65536 + (size_t)(it * 256 + tid) * 16;
            if (o < SZ_FL) *(v4u*)(p + (size_t)(it * 256 + tid) * 16) = s;
        }
    }
}

__global__ void __launch_bounds__(256) k_pre2(const u16* __restrict__ Wq, const u16* __restrict__ Wk, const u16* __restrict__ Wv, const u16* __restrict__ Wo,
                                               const u16* __restrict__ ssmk, const u16* __restrict__ ssmD, char* __restrict__ ws) {
    __shared__ __attribute__((aligned(16))) char smem[2 * HP * 2 + 96 * 40 * 2];
    int b = blockIdx.x;
    const int tid = threadIdx.x, lane = tid & 63, wave = tid >> 6;
    if (b < NB_CONV) {
        const int d = b;
        u16* hE = (u16*)smem; u16* hO = hE + HP; u16* kvs = hO + HP;
        const u16* hrow = ssmk + (size_t)d * S_;
        const v4u hv = *(const v4u*)(hrow + 8 * tid);
        const unsigned hn = tid < 255 ? (unsigned)hrow[8 * tid + 8] : 0u;
        const v4u kvv = *(const v4u*)((const u16*)(ws + OFF_KVT) + (size_t)d * S_ + 8 * tid);
        const float Dd = bf2f(ssmD[d]);
        if (tid < 16) { ((unsigned*)hE)[tid] = 0u; }
        if (tid >= 16 && tid < 31) ((unsigned*)hO)[tid - 16] = 0u;
        for (int i = tid; i < 640; i += 256) ((unsigned*)kvs)[i] = 0u;
        *(v4u*)(hE + 32 + 8 * tid) = hv;
        v4u ho = {(hv.x >> 16) | (hv.y << 16), (hv.y >> 16) | (hv.z << 16), (hv.z >> 16) | (hv.w << 16), (hv.w >> 16) | (hn << 16)};
        *(v4u*)(hO + 32 + 8 * tid) = ho;
        if (tid == 0) ((unsigned*)hO)[15] = hv.x << 16;
        *(v4u*)(kvs + (32 + (tid >> 2)) * 40 + 8 * (tid & 3)) = kvv;
        __syncthreads();
        const int m = lane & 15, q = lane >> 4;
        const unsigned* ap = (m & 1) ? (const unsigned*)(hE + m + 1 + 8 * q) : (const unsigned*)(hO + m + 8 * q);
        const int icol = 16 * wave + m;
        const u16* bp = kvs + (32 + icol) * 40 + 8 * q;
        f32x4 acc0 = {0.f, 0.f, 0.f, 0.f}, acc1 = {0.f, 0.f, 0.f, 0.f};
        const int nd = 16 * wave + 16;
#pragma unroll 4
        for (int dl = 0; dl < nd; ++dl) {
            const bf16x8 bfrag = *(const bf16x8*)(bp - dl * 40);
            const unsigned* a = ap + 16 * dl;
            v4u a0 = {a[0], a[1], a[2], a[3]}, a1 = {a[8], a[9], a[10], a[11]};
            acc0 = MFMA16(__builtin_bit_cast(bf16x8, a0), bfrag, acc0);
            acc1 = MFMA16(__builtin_bit_cast(bf16x8, a1), bfrag, acc1);
        }
        u16* yT = (u16*)(ws + OFF_YT) + (size_t)d * S_;
#pragma unroll
        for (int mt = 0; mt < 2; ++mt) {
            const v2u kr = *(const v2u*)(kvs + (32 + icol) * 40 + 28 - 16 * mt - 4 * q);
            const float k3 = __uint_as_float(kr.x << 16), k2 = __uint_as_float(kr.x & 0xffff0000u);
            const float k1 = __uint_as_float(kr.y << 16), k0 = __uint_as_float(kr.y & 0xffff0000u);
            const f32x4 a = mt ? acc1 : acc0;
            const float y0 = rb(rb(a[0]) + rb(k0 * Dd)), y1 = rb(rb(a[1]) + rb(k1 * Dd));
            const float y2 = rb(rb(a[2]) + rb(k2 * Dd)), y3 = rb(rb(a[3]) + rb(k3 * Dd));
            v2u o = {pack2(y0, y1), pack2(y2, y3)};
            *(v2u*)(yT + 32 * icol + 16 * mt + 4 * q) = o;
        }
        return;
    }
    b -= NB_CONV;
    {
        unsigned (*T)[33] = (unsigned (*)[33])smem;
        if (b < 3 * 448) {
            const int w = b / 448, t = b % 448;
            const u16* src = w == 0 ? Wq : (w == 1 ? Wk : Wv);
            const int r0 = (t / 64) * 64, c0 = (t % 64) * 64;
            tile_load(src, 4096, r0, c0, T);
            __syncthreads();
            tile_store(T, (u16*)(ws + OFF_WQKVT), 448, w * 4096 + c0, r0);
            return;
        }
        b -= 3 * 448;
        const int r0 = (b / 7) * 64, c0 = (b % 7) * 64;
        tile_load(Wo, 448, r0, c0, T);
        __syncthreads();
        tile_store(T, (u16*)(ws + OFF_WOT), 4096, c0, r0);
        u16* wof = (u16*)(ws + OFF_WOF);
#pragma unroll
        for (int it = 0; it < 2; ++it) {
            const int i = tid + 256 * it;
            const int dli = i & 63, eq = i >> 6;
            const int e0 = r0 + eq * 8, dd = c0 + dli;
            const int h = e0 >> 8, ee = e0 & 255, ks = ee >> 4, hh = (ee >> 3) & 1, dt = dd >> 5, r = dd & 31;
            v4u v = {T[dli][eq * 4], T[dli][eq * 4 + 1], T[dli][eq * 4 + 2], T[dli][eq * 4 + 3]};
            *(v4u*)(wof + ((((size_t)h * 14 + dt) * 16 + ks) * 64 + hh * 32 + r) * 8) = v;
        }
    }
}

DI float wave_sum(float v) {
#pragma unroll
    for (int m = 32; m >= 1; m >>= 1) v += __shfl_xor(v, m);
    return v;
}
DI void layernorm7(const float (&x)[7], float (&o)[7]) {
    float s = 0.f;
#pragma unroll
    for (int i = 0; i < 7; ++i) s += x[i];
    s = wave_sum(s);
    const float mean = s * (1.0f / 448.0f);
    float q = 0.f;
#pragma unroll
    for (int i = 0; i < 7; ++i) { float dlt = x[i] - mean; q += dlt * dlt; }
    q = wave_sum(q);
    const float m = rb(mean), v = rb(q * (1.0f / 448.0f));
    const float den = rb(sqrtf(rb(v + 1e-5f)));
#pragma unroll
    for (int i = 0; i < 7; ++i) o[i] = rb(rb(x[i] - m) / den);
}

__global__ void __launch_bounds__(512) k_stage1(const u16* __restrict__ x0, char* __restrict__ ws) {
    __shared__ u16 yt[8][456];
    const int t0 = blockIdx.x * 8, tid = threadIdx.x, lane = tid & 63, wave = tid >> 6;
    const u16* yT = (const u16*)(ws + OFF_YT);
    const int t = t0 + wave;
    float x[7], ln[7];
#pragma unroll
    for (int i = 0; i < 7; ++i) x[i] = bf2f(x0[(size_t)t * D_ + lane + 64 * i]);
    if (tid < 448) {
        const v4u w = *(const v4u*)(yT + (size_t)tid * S_ + t0);
        yt[0][tid] = (u16)(w.x & 0xffff); yt[1][tid] = (u16)(w.x >> 16);
        yt[2][tid] = (u16)(w.y & 0xffff); yt[3][tid] = (u16)(w.y >> 16);
        yt[4][tid] = (u16)(w.z & 0xffff); yt[5][tid] = (u16)(w.z >> 16);
        yt[6][tid] = (u16)(w.w & 0xffff); yt[7][tid] = (u16)(w.w >> 16);
    }
    layernorm7(x, ln);
    __syncthreads();
    u16* x1 = (u16*)(ws + OFF_X1); u16* hb = (u16*)(ws + OFF_HB); u16* yr = (u16*)(ws + OFF_Y);
#pragma unroll
    for (int i = 0; i < 7; ++i) x[i] = rb(x[i] + rb(ln[i] * bf2f(yt[wave][lane + 64 * i])));
    layernorm7(x, ln);
#pragma unroll
    for (int i = 0; i < 7; ++i) {
        x1[(size_t)t * D_ + lane + 64 * i] = f2bf(x[i]); hb[(size_t)t * D_ + lane + 64 * i] = f2bf(ln[i]);
        yr[(size_t)t * D_ + lane + 64 * i] = yt[wave][lane + 64 * i];
    }
}

constexpr unsigned SENT = 0xFFFFFFFFu;
DI bool okv(v4u v) { return v.x != SENT && v.y != SENT && v.z != SENT && v.w != SENT; }
DI unsigned san(unsigned w) { return w == SENT ? 0x7FC07FC0u : w; }
DI v4u san4(v4u v) { v4u o = {san(v.x), san(v.y), san(v.z), san(v.w)}; return o; }
constexpr int SPIN_CAP = 1 << 16;
constexpr int WAIT_CAP = 1 << 16;
constexpr int MS = 456;
constexpr int SSR = 44;
constexpr int GS = 72;
constexpr int OS = 264;
constexpr int LDS_BYTES = 80 * 1024;

DI float xmax16(float v) { auto r = __builtin_amdgcn_permlane16_swap(__float_as_uint(v), __float_as_uint(v), false, false); return fmaxf(__uint_as_float(r[0]), __uint_as_float(r[1])); }
DI float xmax32(float v) { auto r = __builtin_amdgcn_permlane32_swap(__float_as_uint(v), __float_as_uint(v), false, false); return fmaxf(__uint_as_float(r[0]), __uint_as_float(r[1])); }
DI float xsum16(float v) { auto r = __builtin_amdgcn_permlane16_swap(__float_as_uint(v), __float_as_uint(v), false, false); return __uint_as_float(r[0]) + __uint_as_float(r[1]); }
DI float xsum32(float v) { auto r = __builtin_amdgcn_permlane32_swap(__float_as_uint(v), __float_as_uint(v), false, false); return __uint_as_float(r[0]) + __uint_as_float(r[1]); }
template <int CTRL> DI float dpp_f(float v) { return __uint_as_float(__builtin_amdgcn_update_dpp(0u, __float_as_uint(v), CTRL, 0xF, 0xF, false)); }
DI float rowsum16(float v) {
    v += dpp_f<0x128>(v);
    v += dpp_f<0x124>(v);
    v += dpp_f<0x122>(v);
    v += dpp_f<0x121>(v);
    return v;
}

DI float rowmax16(float v) {
    v = fmaxf(v, dpp_f<0x128>(v));
    v = fmaxf(v, dpp_f<0x124>(v));
    v = fmaxf(v, dpp_f<0x122>(v));
    v = fmaxf(v, dpp_f<0x121>(v));
    return v;
}
#define TR(i)
#define STAMP(i)

#define PSTAG 12
#define POLL2(N, v, LDSTMT, ERRCODE) { \
    v4u pa_[N], pb_[N]; \
    { v4u (&d_)[N] = pa_; LDSTMT } \
    __builtin_amdgcn_s_sleep(PSTAG); \
    int sp_ = 0; \
    for (;;) { \
        asm volatile("" ::: "memory"); \
        { v4u (&d_)[N] = pb_; LDSTMT } \
        asm volatile("" ::: "memory"); \
        { bool ok_ = true; _Pragma("unroll") for (int i_ = 0; i_ < N; ++i_) ok_ = ok_ && okv(pa_[i_]); \
          if (__all(ok_)) { _Pragma("unroll") for (int i_ = 0; i_ < N; ++i_) v[i_] = pa_[i_]; break; } } \
        asm volatile("" ::: "memory"); \
        { v4u (&d_)[N] = pa_; LDSTMT } \
        asm volatile("" ::: "memory"); \
        { bool ok_ = true; _Pragma("unroll") for (int i_ = 0; i_ < N; ++i_) ok_ = ok_ && okv(pb_[i_]); \
          if (__all(ok_)) { _Pragma("unroll") for (int i_ = 0; i_ < N; ++i_) v[i_] = pb_[i_]; break; } } \
        if (++sp_ >= SPIN_CAP) { __hip_atomic_store(errw, ERRCODE, __ATOMIC_RELAXED, __HIP_MEMORY_SCOPE_AGENT); _Pragma("unroll") for (int i_ = 0; i_ < N; ++i_) v[i_] = pa_[i_]; break; } \
    } }

DI unsigned* flag_ptr(char* ws, int idx) { return (unsigned*)(ws + OFF_FL + (size_t)idx * 64); }
DI unsigned flag_load(char* ws, int idx) { return __hip_atomic_load(flag_ptr(ws, idx), __ATOMIC_RELAXED, __HIP_MEMORY_SCOPE_AGENT); }
DI void flag_add(char* ws, int idx) { __hip_atomic_fetch_add(flag_ptr(ws, idx), 1u, __ATOMIC_RELAXED, __HIP_MEMORY_SCOPE_AGENT); }
DI void wait_flag(char* ws, int idx, unsigned target) {
    if (threadIdx.x == 0) {
        int sp = 0;
        for (; sp < WAIT_CAP; ++sp) {
            if (flag_load(ws, idx) >= target) break;
            if (flag_load(ws, FL_ERR) != 0u) break;
            __builtin_amdgcn_s_sleep(8);
        }
        if (sp == WAIT_CAP) __hip_atomic_store(flag_ptr(ws, FL_ERR), 5u, __ATOMIC_RELAXED, __HIP_MEMORY_SCOPE_AGENT);
    }
    __syncthreads();
}
DI void signal_flag(char* ws, int idx) {
    __builtin_amdgcn_s_waitcnt(0x0F70);
    __syncthreads();
    if (threadIdx.x == 0) flag_add(ws, idx);
}

DI void task_qkv(char* ws, char* smem, int layer, int G, int nb, int tid) {
    u16* sW = (u16*)smem;
    u16* sH = sW + 2 * 128 * GS;
    const int lane = tid & 63, wave = tid >> 6;
    const int n0 = nb * 128, t0 = G * 128;
    const u16* WT = (const u16*)(ws + OFF_WQKVT) + (size_t)n0 * D_;
    auto rH = __builtin_amdgcn_make_buffer_rsrc(ws + OFF_HB + ((size_t)layer * S_ + t0) * D_ * 2, 0, 128 * D_ * 2, 0x00020000);
    const bool isV = nb >= 64;
    const int wr = wave >> 1, wc = wave & 1;
    const int r = lane & 31, hh = lane >> 5;
    f32x16 acc[2];
#pragma unroll
    for (int b = 0; b < 2; ++b)
#pragma unroll
        for (int i = 0; i < 16; ++i) acc[b][i] = 0.f;
    v4u pw[7][2], ph[7][2];
#pragma unroll
    for (int kb = 0; kb < 7; ++kb)
#pragma unroll
        for (int i = 0; i < 2; ++i) {
            int c = tid + 512 * i, row = c >> 3, kq = c & 7;
            ph[kb][i] = __builtin_amdgcn_raw_buffer_load_b128(rH, (row * D_ + kb * 64 + kq * 8) * 2, 0, 16);
            pw[kb][i] = *(const v4u*)(WT + (size_t)row * D_ + kb * 64 + kq * 8);
        }
#pragma unroll
    for (int i = 0; i < 2; ++i) { int c = tid + 512 * i, row = c >> 3, kq = c & 7; *(v4u*)(sW + row * GS + kq * 8) = pw[0][i]; *(v4u*)(sH + row * GS + kq * 8) = ph[0][i]; }
    __syncthreads();
#pragma unroll
    for (int kb = 0; kb < 7; ++kb) {
        const int cur = (kb & 1) * 128 * GS, nxt = ((kb + 1) & 1) * 128 * GS;
        if (kb < 6) {
#pragma unroll
            for (int i = 0; i < 2; ++i) { int c = tid + 512 * i, row = c >> 3, kq = c & 7; *(v4u*)(sW + nxt + row * GS + kq * 8) = pw[kb + 1][i]; *(v4u*)(sH + nxt + row * GS + kq * 8) = ph[kb + 1][i]; }
        }
#pragma unroll
        for (int ks = 0; ks < 4; ++ks) {
            bf16x8 fw = *(const bf16x8*)(sW + cur + (wr * 32 + r) * GS + ks * 16 + hh * 8);
            bf16x8 fh[2];
#pragma unroll
            for (int b = 0; b < 2; ++b) fh[b] = *(const bf16x8*)(sH + cur + (wc * 64 + b * 32 + r) * GS + ks * 16 + hh * 8);
            if (!isV) {
#pragma unroll
                for (int b = 0; b < 2; ++b) acc[b] = MFMA32(fw, fh[b], acc[b]);
            } else {
#pragma unroll
                for (int b = 0; b < 2; ++b) acc[b] = MFMA32(fh[b], fw, acc[b]);
            }
        }
        __syncthreads();
    }
    u16* sT = (u16*)smem;
    if (!isV) {
#pragma unroll
        for (int b = 0; b < 2; ++b) {
            int t = wc * 64 + b * 32 + r;
#pragma unroll
            for (int g4 = 0; g4 < 4; ++g4) {
                int n = wr * 32 + 8 * g4 + 4 * hh;
                v2u v = {pack2(acc[b][4 * g4], acc[b][4 * g4 + 1]), pack2(acc[b][4 * g4 + 2], acc[b][4 * g4 + 3])};
                *(v2u*)(sT + t * 136 + n) = v;
            }
        }
        __syncthreads();
        auto rD = __builtin_amdgcn_make_buffer_rsrc(ws + (nb < 32 ? OFF_Q : OFF_K) + ((size_t)layer * S_ + t0) * NH_ * 2, 0, 128 * NH_ * 2, 0x00020000);
        const int hd = (nb >> 1) & 15, c0 = (nb & 1) * 16;
#pragma unroll
        for (int k = 0; k < 4; ++k) {
            int i = tid + 512 * k, sg = i >> 9, c = (i >> 5) & 15, row = i & 31;
            v4u v = *(const v4u*)(sT + (sg * 32 + row) * 136 + c * 8);
            __builtin_amdgcn_raw_buffer_store_b128(v, rD, (((sg * 16 + hd) * 32 + c0 + c) * 32 + row) * 16, 0, 16);
        }
    } else {
#pragma unroll
        for (int b = 0; b < 2; ++b) {
            int n = wr * 32 + r;
#pragma unroll
            for (int g4 = 0; g4 < 4; ++g4) {
                int t = wc * 64 + b * 32 + 8 * g4 + 4 * hh;
                v2u v = {pack2(acc[b][4 * g4], acc[b][4 * g4 + 1]), pack2(acc[b][4 * g4 + 2], acc[b][4 * g4 + 3])};
                *(v2u*)(sT + n * 136 + t) = v;
            }
        }
        __syncthreads();
        auto rD = __builtin_amdgcn_make_buffer_rsrc(ws + OFF_VT + ((size_t)layer * S_ + t0) * NH_ * 2, 0, 128 * NH_ * 2, 0x00020000);
        const int hd = (nb >> 1) & 15, e0 = (nb & 1) * 128;
#pragma unroll
        for (int k = 0; k < 4; ++k) {
            int i = tid + 512 * k, sg = i >> 9, kc = (i >> 7) & 3, e = i & 127;
            v4u v = *(const v4u*)(sT + e * 136 + sg * 32 + kc * 8);
            __builtin_amdgcn_raw_buffer_store_b128(v, rD, (((sg * 16 + hd) * 4 + kc) * 256 + e0 + e) * 16, 0, 16);
        }
    }
}

DI void task_att(char* ws, char* smem, int layer, int G, int h, int tid, unsigned long long* trc) {
    u16* so = (u16*)smem;
    v4u* sP = (v4u*)(smem + 128 * OS * 2);
    const int lane = tid & 63, wave = tid >> 6;
    const int sg = wave & 3, role = wave >> 2;
    const int seg = G * 4 + sg;
    const int r = lane & 31, hh = lane >> 5;
    const int pr = (r & 19) | ((r & 4) << 1) | ((r & 8) >> 1);
    const size_t sh = ((size_t)layer * NSEG_ + seg) * 16 + h;
    const u16* WoF = (const u16*)(ws + OFF_WOF) + (size_t)h * 14 * 8192;
    const v4u zero4 = {0u, 0u, 0u, 0u};
    bf16x8 wf[16];
    v4u va[8][2], vm[8];
    if (role == 0) {
        auto rQ = __builtin_amdgcn_make_buffer_rsrc(ws + OFF_Q + sh * 8192 * 2, 0, 8192 * 2, 0x00020000);
        auto rK = __builtin_amdgcn_make_buffer_rsrc(ws + OFF_K + sh * 8192 * 2, 0, 8192 * 2, 0x00020000);
        auto rKM = __builtin_amdgcn_make_buffer_rsrc(ws + OFF_KMEM + sh * 2048 * 2, 0, 2048 * 2, 0x00020000);
        f32x16 s1, s0;
#pragma unroll
        for (int i = 0; i < 16; ++i) { s1[i] = 0.f; s0[i] = 0.f; }
        v4u qf[16], kf[16], mf[16];
#pragma unroll
        for (int ks = 0; ks < 16; ++ks) {
            qf[ks] = __builtin_amdgcn_raw_buffer_load_b128(rQ, ((2 * ks + hh) * 32 + r) * 16, 0, 16);
            kf[ks] = __builtin_amdgcn_raw_buffer_load_b128(rK, ((2 * ks + hh) * 32 + pr) * 16, 0, 16);
            mf[ks] = __builtin_amdgcn_raw_buffer_load_b128(rKM, ((2 * ks + hh) * 8 + (pr & 7)) * 16, 0, 16);
        }
#pragma unroll
        for (int ks = 0; ks < 16; ++ks) {
            s1 = MFMA32(__builtin_bit_cast(bf16x8, kf[ks]), __builtin_bit_cast(bf16x8, qf[ks]), s1);
            v4u m = pr < 8 ? mf[ks] : zero4;
            s0 = MFMA32(__builtin_bit_cast(bf16x8, m), __builtin_bit_cast(bf16x8, qf[ks]), s0);
        }
        TR(3)
        float sc[16], sm[8];
        float mx = -3.0e38f;
#pragma unroll
        for (int reg = 0; reg < 16; ++reg) {
            int key = 16 * (reg >> 3) + 8 * hh + (reg & 7);
            float v = rb(s1[reg]) * 0.0625f;
            v = (key <= r) ? v : -1000000000.0f;
            sc[reg] = v; mx = fmaxf(mx, v);
        }
#pragma unroll
        for (int reg = 0; reg < 8; ++reg) { float v = rb(s0[reg]) * 0.0625f; sm[reg] = v; if (hh == 0) mx = fmaxf(mx, v); }
        mx = xmax32(mx);
        float sum = 0.f;
#pragma unroll
        for (int reg = 0; reg < 16; ++reg) { sc[reg] = __expf(sc[reg] - mx); sum += sc[reg]; }
#pragma unroll
        for (int reg = 0; reg < 8; ++reg) { sm[reg] = hh == 0 ? __expf(sm[reg] - mx) : 0.f; sum += sm[reg]; }
        sum = xsum32(sum);
        const float inv = 1.0f / sum;
#pragma unroll
        for (int s = 0; s < 2; ++s) {
            v4u w = {pack2(sc[8 * s] * inv, sc[8 * s + 1] * inv), pack2(sc[8 * s + 2] * inv, sc[8 * s + 3] * inv), pack2(sc[8 * s + 4] * inv, sc[8 * s + 5] * inv), pack2(sc[8 * s + 6] * inv, sc[8 * s + 7] * inv)};
            sP[(sg * 3 + s) * 64 + lane] = w;
        }
        { v4u w = {pack2(sm[0] * inv, sm[1] * inv), pack2(sm[2] * inv, sm[3] * inv), pack2(sm[4] * inv, sm[5] * inv), pack2(sm[6] * inv, sm[7] * inv)}; sP[(sg * 3 + 2) * 64 + lane] = w; }
        TR(4)
    } else {
        auto rV = __builtin_amdgcn_make_buffer_rsrc(ws + OFF_VT + sh * 8192 * 2, 0, 8192 * 2, 0x00020000);
        auto rVM = __builtin_amdgcn_make_buffer_rsrc(ws + OFF_VMEMT + sh * 2048 * 2, 0, 2048 * 2, 0x00020000);
#pragma unroll
        for (int et = 0; et < 8; ++et) {
#pragma unroll
            for (int s = 0; s < 2; ++s) va[et][s] = __builtin_amdgcn_raw_buffer_load_b128(rV, ((2 * s + hh) * 256 + et * 32 + r) * 16, 0, 16);
            vm[et] = __builtin_amdgcn_raw_buffer_load_b128(rVM, (et * 32 + r) * 16, 0, 16);
        }
    }
#pragma unroll
    for (int ks = 0; ks < 16; ++ks) wf[ks] = *(const bf16x8*)(WoF + ((size_t)(wave * 16 + ks) * 64 + lane) * 8);
    __syncthreads();
    if (role == 1) {
        bf16x8 pf[3];
#pragma unroll
        for (int s = 0; s < 3; ++s) pf[s] = __builtin_bit_cast(bf16x8, sP[(sg * 3 + s) * 64 + lane]);
#pragma unroll
        for (int et = 0; et < 8; ++et) {
            f32x16 o;
#pragma unroll
            for (int i = 0; i < 16; ++i) o[i] = 0.f;
#pragma unroll
            for (int s = 0; s < 2; ++s) o = MFMA32(__builtin_bit_cast(bf16x8, va[et][s]), pf[s], o);
            o = MFMA32(__builtin_bit_cast(bf16x8, hh == 0 ? vm[et] : zero4), pf[2], o);
#pragma unroll
            for (int g4 = 0; g4 < 4; ++g4) {
                v2u v = {pack2(o[4 * g4], o[4 * g4 + 1]), pack2(o[4 * g4 + 2], o[4 * g4 + 3])};
                *(v2u*)(so + (sg * 32 + r) * OS + et * 32 + 8 * g4 + 4 * hh) = v;
            }
        }
    }
    __syncthreads();
    TR(5)
    auto rP = __builtin_amdgcn_make_buffer_rsrc(ws + OFF_PART + ((size_t)h * S_ + G * 128) * D_ * 4, 0, 128 * D_ * 4, 0x00020000);
#pragma unroll
    for (int rnd = 0; rnd < 2; ++rnd) {
        const int dt = wave + 8 * rnd;
        if (dt < 14) {
            f32x16 acc[4];
#pragma unroll
            for (int b = 0; b < 4; ++b)
#pragma unroll
                for (int i = 0; i < 16; ++i) acc[b][i] = 0.f;
            bf16x8 wc[16];
#pragma unroll
            for (int ks = 0; ks < 16; ++ks) wc[ks] = wf[ks];
            if (rnd == 1) { TR(6) }
            if (rnd == 0 && wave + 8 < 14) {
#pragma unroll
                for (int ks = 0; ks < 16; ++ks) wf[ks] = *(const bf16x8*)(WoF + ((size_t)((wave + 8) * 16 + ks) * 64 + lane) * 8);
            }
#pragma unroll
            for (int ks = 0; ks < 16; ++ks) {
#pragma unroll
                for (int b = 0; b < 4; ++b) {
                    bf16x8 of = *(const bf16x8*)(so + (b * 32 + r) * OS + ks * 16 + hh * 8);
                    acc[b] = MFMA32(of, wc[ks], acc[b]);
                }
            }
#pragma unroll
            for (int b = 0; b < 4; ++b)
#pragma unroll
                for (int reg = 0; reg < 16; ++reg) {
                    int t = b * 32 + (reg & 3) + 8 * (reg >> 2) + 4 * hh;
                    __builtin_amdgcn_raw_buffer_store_b32(__float_as_uint(acc[b][reg]), rP, (t * D_ + dt * 32 + r) * 4, 0, 16);
                }
        }
    }
}

DI void layernorm8(const float (&x)[8], float (&o)[8], bool on) {
    float s = 0.f;
#pragma unroll
    for (int i = 0; i < 8; ++i) s += x[i];
    s = wave_sum(s);
    const float mean = s * (1.0f / 448.0f);
    float q = 0.f;
#pragma unroll
    for (int i = 0; i < 8; ++i) { float dlt = x[i] - mean; q += on ? dlt * dlt : 0.f; }
    q = wave_sum(q);
    const float m = rb(mean), v = rb(q * (1.0f / 448.0f));
    const float den = rb(sqrtf(rb(v + 1e-5f)));
#pragma unroll
    for (int i = 0; i < 8; ++i) o[i] = on ? rb(rb(x[i] - m) / den) : 0.f;
}
DI void unpack8(v4u w, float (&x)[8]) {
    x[0] = __uint_as_float(w.x << 16); x[1] = __uint_as_float(w.x & 0xffff0000u); x[2] = __uint_as_float(w.y << 16); x[3] = __uint_as_float(w.y & 0xffff0000u);
    x[4] = __uint_as_float(w.z << 16); x[5] = __uint_as_float(w.z & 0xffff0000u); x[6] = __uint_as_float(w.w << 16); x[7] = __uint_as_float(w.w & 0xffff0000u);
}
DI v4u pack8(const float (&x)[8]) { v4u w = {pack2(x[0], x[1]), pack2(x[2], x[3]), pack2(x[4], x[5]), pack2(x[6], x[7])}; return w; }
DI void task_red(char* ws, u16* __restrict__ out, int layer, int rb8, int tid) {
    const int lane = tid & 63, wave = tid >> 6;
    const bool on = lane < 56;
    const int lc = on ? lane : 0;
    auto rP = __builtin_amdgcn_make_buffer_rsrc(ws + OFF_PART, 0, (unsigned)((size_t)16 * S_ * D_ * 4), 0x00020000);
    auto rX = __builtin_amdgcn_make_buffer_rsrc(ws + (layer == 0 ? OFF_X1 : OFF_X3), 0, S_ * D_ * 2, 0x00020000);
    auto rX3 = __builtin_amdgcn_make_buffer_rsrc(ws + OFF_X3, 0, S_ * D_ * 2, 0x00020000);
    auto rH3 = __builtin_amdgcn_make_buffer_rsrc(ws + OFF_HB + (size_t)S_ * D_ * 2, 0, S_ * D_ * 2, 0x00020000);
    const u16* yr = (const u16*)(ws + OFF_Y);
    const int t = rb8 * 8 + wave;
    f32x4 pa[16], pb[16];
#pragma unroll
    for (int h = 0; h < 16; ++h) {
        pa[h] = __builtin_bit_cast(f32x4, __builtin_amdgcn_raw_buffer_load_b128(rP, ((h * S_ + t) * D_ + lc * 8) * 4, 0, 16));
        pb[h] = __builtin_bit_cast(f32x4, __builtin_amdgcn_raw_buffer_load_b128(rP, ((h * S_ + t) * D_ + lc * 8 + 4) * 4, 0, 16));
    }
    v4u xw = __builtin_amdgcn_raw_buffer_load_b128(rX, (t * D_ + lc * 8) * 2, 0, 16);
    v4u yw = *(const v4u*)(yr + (size_t)t * D_ + lc * 8);
    float x[8], ln[8], y[8];
    unpack8(xw, x); unpack8(yw, y);
#pragma unroll
    for (int i = 0; i < 4; ++i) {
        float sa = 0.f, sb = 0.f;
#pragma unroll
        for (int h = 0; h < 16; ++h) { sa += pa[h][i]; sb += pb[h][i]; }
        x[i] = on ? rb(x[i] + rb(sa)) : 0.f; x[4 + i] = on ? rb(x[4 + i] + rb(sb)) : 0.f;
    }
    if (layer == 1) {
        if (on) *(v4u*)(out + (size_t)t * D_ + lane * 8) = pack8(x);
    } else {
        layernorm8(x, ln, on);
#pragma unroll
        for (int i = 0; i < 8; ++i) x[i] = on ? rb(x[i] + rb(ln[i] * y[i])) : 0.f;
        layernorm8(x, ln, on);
        if (on) {
            __builtin_amdgcn_raw_buffer_store_b128(pack8(x), rX3, (t * D_ + lane * 8) * 2, 0, 16);
            __builtin_amdgcn_raw_buffer_store_b128(pack8(ln), rH3, (t * D_ + lane * 8) * 2, 0, 16);
        }
    }
}

#define BLD(r, vo, so) __builtin_amdgcn_raw_buffer_load_b128((r), (vo), (so), 16)
#define BST(v, r, vo, so) __builtin_amdgcn_raw_buffer_store_b128((v), (r), (vo), (so), 16)
#define BST64(v, r, vo, so) __builtin_amdgcn_raw_buffer_store_b64((v), (r), (vo), (so), 16)
DI void chain_role(const u16* __restrict__ mem0, char* __restrict__ ws, char* smem, int start1, int nticks) {
    u16* sMem = (u16*)smem;
    u16* sQ = sMem + 16 * MS;
    u16* sKm = sQ + 16 * 72;
    u16* sVT = sKm + 16 * 72;
    float* sS = (float*)(sVT + 256 * 24);
    u16* sO = (u16*)(sS + 16 * SSR);
    u16* sM = sO + 16 * 264;
    u16* sP = sM + 128;
    const int wg = blockIdx.x, tid = threadIdx.x, lane = tid & 63;
    const int wave = __builtin_amdgcn_readfirstlane(tid >> 6);
    const int h = wg >> 2, g = wg & 3;
    const int r16 = lane & 15, kg = lane >> 4;
    const int start0 = 0;
    const u16* WT = (const u16*)(ws + OFF_WQKVT);
    const u16* WoT = (const u16*)(ws + OFF_WOT);
    auto rX = __builtin_amdgcn_make_buffer_rsrc(ws + OFF_XA, 0, (unsigned)(OFF_FL - OFF_XA), 0x00020000);
    auto rD = __builtin_amdgcn_make_buffer_rsrc(ws + OFF_K, 0, (unsigned)(OFF_PART - OFF_K), 0x00020000);
    constexpr unsigned XB0 = (unsigned)(OFF_XB - OFF_XA), XC0 = (unsigned)(OFF_XC - OFF_XA);
    constexpr unsigned VT0 = (unsigned)(OFF_VT - OFF_K), KM0 = (unsigned)(OFF_KMEM - OFF_K), VM0 = (unsigned)(OFF_VMEMT - OFF_K);
    unsigned* errw = flag_ptr(ws, FL_ERR);
    const v4u sent4 = {SENT, SENT, SENT, SENT};
    const bf16x8 zero8 = {0, 0, 0, 0, 0, 0, 0, 0};
    const v4u zero4 = {0u, 0u, 0u, 0u};

    bf16x8 wreg[30];
    {
        const int w4 = wave & 3;
        const int m0 = wave < 4 ? 0 : 2;
#pragma unroll
        for (int ks = 0; ks < 14; ++ks)
            wreg[ks] = *(const bf16x8*)(WT + (size_t)(m0 * 4096 + h * DH_ + g * 64 + w4 * 16 + r16) * D_ + ks * 32 + kg * 8);
        if (wave < 4) {
#pragma unroll
            for (int ks = 0; ks < 14; ++ks)
                wreg[14 + ks] = *(const bf16x8*)(WT + (size_t)(1 * 4096 + h * DH_ + g * 64 + w4 * 16 + r16) * D_ + ks * 32 + kg * 8);
            wreg[28] = zero8; wreg[29] = zero8;
        } else {
#pragma unroll
            for (int tl = 0; tl < 2; ++tl)
#pragma unroll
                for (int ks = 0; ks < 8; ++ks) {
                    int dt = 2 * w4 + tl;
                    wreg[14 + tl * 8 + ks] = (dt < 7) ? *(const bf16x8*)(WoT + (size_t)(g * 112 + dt * 16 + r16) * NH_ + h * DH_ + ks * 32 + kg * 8) : zero8;
                }
        }
    }
    for (int i = tid; i < 16 * MS; i += 512) sMem[i] = 0;
    for (int i = tid; i < 256 * 24; i += 512) sVT[i] = 0;

    const int cr = r16 >> 3;
    const int ck = kg >> 1;
    const unsigned vT = (unsigned)tid * 16u;
    const int vu = tid - 320;
    const bool v3 = vu < 128;
    unsigned vKV, vAv = 0;
    if (wave < 4) vKV = KM0 + (unsigned)((((cr * NSEG_) * 16 * 32 + wave * 2 + (kg >> 1)) * 8 + (r16 & 7)) * 16 + (kg & 1) * 8);
    else {
        const int e = (wave - 4) * 16 + r16;
        vKV = VM0 + (unsigned)((((ck * NSEG_) * NH_ + e) * 8 + 4 * (kg & 1)) * 2);
        vAv = (unsigned)(320 * 16 + e * 32 + kg * 8);
    }
    unsigned vP0 = 0, vP1 = 0, vP2 = 0;
    if (wave >= 5) {
        auto f = [&](int x) { return (unsigned)(((x >> 7) * XA_UNITS + 320 + (x & 127)) * 16); };
        vP0 = f(vu); vP1 = f(vu + 192); vP2 = v3 ? f(vu + 384) : vP1;
    }
    const unsigned vBs = XB0 + (unsigned)(((r16 * GG) * 16 * 32 + kg) * 16);
    const unsigned vBp = XB0 + (unsigned)((r16 * 32 + wave * 4 + kg) * 16);
    const int pc0 = tid / 14, pk0 = tid % 14, pc1 = (tid + 512) / 14, pk1 = (tid + 512) % 14;
    const unsigned vPub0 = XC0 + (unsigned)((pc0 * 896 + pk0) * 16), vPub1 = XC0 + (unsigned)((pc1 * 896 + pk1) * 16);
    const bool pub1 = tid < NCH * 14 - 512;
    const int kxc = (wave - 1) >> 1;
    const unsigned vKX = (unsigned)((kg * 32 + ((wave - 1) & 1) * 16 + r16) * 16);
    const unsigned vVX = VT0 + (unsigned)((kg * 256 + wave * 32 + r16) * 16);
    const bool svalid = (wave == 0) ? ((kg >> 1) == cr) : (kxc == cr);

    bf16x8 kxf[2] = {zero8, zero8};
    bf16x8 vxf[2][2];
    auto load_x = [&](int T) {
        int t0 = T - start0, t1 = T - start1;
        t0 = t0 < 0 ? 0 : (t0 > NSEG_ - 1 ? NSEG_ - 1 : t0);
        t1 = t1 < 0 ? 0 : (t1 > NSEG_ - 1 ? NSEG_ - 1 : t1);
        const unsigned b0 = (unsigned)((0 * NSEG_ + t0) * 16 + h) * 16384u, b1 = (unsigned)((1 * NSEG_ + t1) * 16 + h) * 16384u;
        if (wave >= 1 && wave <= 4) {
            const unsigned so = (kxc ? b1 : b0) + (unsigned)(g * 8 * 32 * 16);
            kxf[0] = __builtin_bit_cast(bf16x8, BLD(rD, vKX, so));
            kxf[1] = __builtin_bit_cast(bf16x8, BLD(rD, vKX, so + 4 * 32 * 16));
        }
        vxf[0][0] = __builtin_bit_cast(bf16x8, BLD(rD, vVX, b0));
        vxf[0][1] = __builtin_bit_cast(bf16x8, BLD(rD, vVX, b0 + 256));
        vxf[1][0] = __builtin_bit_cast(bf16x8, BLD(rD, vVX, b1));
        vxf[1][1] = __builtin_bit_cast(bf16x8, BLD(rD, vVX, b1 + 256));
    };
    auto wait_qkv = [&](int T) {
#pragma unroll
        for (int c = 0; c < 2; ++c) {
            const int tc = T - (c ? start1 : start0);
            if (tc >= 0 && tc < NSEG_ && (tc & 3) == 0) {
                int sp = 0;
                for (; sp < WAIT_CAP; ++sp) {
                    if (flag_load(ws, FL_QKV + (c * 16 + (tc >> 2)) * 16 + h) >= 6u) break;
                    if (flag_load(ws, FL_ERR) != 0u) break;
                    __builtin_amdgcn_s_sleep(16);
                }
                if (sp == WAIT_CAP) __hip_atomic_store(errw, 6u, __ATOMIC_RELAXED, __HIP_MEMORY_SCOPE_AGENT);
            }
        }
    };
    wait_qkv(0);
    load_x(0);
    __syncthreads();

    for (int T = 0; T < nticks; ++T) {
        const int sl = T & (RING - 1), sn = (T + 1) & (RING - 1), spv = (T - 1) & (RING - 1);
        const int tc0 = T - start0, tc1 = T - start1;
        const bool act0 = tc0 >= 0 && tc0 < NSEG_;
        const bool act1 = tc1 >= 0 && tc1 < NSEG_;
        const unsigned sC = XC0 + (unsigned)((sl * NCH + wg) * 896 * 16);
        const bool doC = T > 0 && tid < 448;
        {
            v4u v0 = zero4, v1 = zero4;
            if (T > 0 && wave < 7) BST(sent4, rX, vBp, (unsigned)((spv * NCH + wg) * 16 * 32 * 16));
            asm volatile("" ::: "memory");
            if (doC) {
                int sp = 0;
                do {
                    asm volatile("" ::: "memory");
                    v0 = BLD(rX, vT, sC);
                    v1 = BLD(rX, vT, sC + 448 * 16);
                    if (++sp == SPIN_CAP) { __hip_atomic_store(errw, 1u, __ATOMIC_RELAXED, __HIP_MEMORY_SCOPE_AGENT); break; }
                } while (!(okv(v0) && okv(v1)));
            }
            if (tid < 448) {
                const int row = tid / 56, c = tid % 56;
                if (!act0) v0 = zero4;
                if (!act1) v1 = zero4;
                if ((act0 && tc0 == 0) || (act1 && tc1 == 0)) {
                    v4u m = *(const v4u*)(mem0 + (size_t)row * D_ + c * 8);
                    if (act0 && tc0 == 0) v0 = m;
                    if (act1 && tc1 == 0) v1 = m;
                }
                *(v4u*)(sMem + row * MS + c * 8) = v0;
                *(v4u*)(sMem + (8 + row) * MS + c * 8) = v1;
            }
        }
        __syncthreads();
        STAMP(0)
        const unsigned sA0 = (unsigned)(((sl * NCH + h * GG) * GG + g) * XA_UNITS * 16);
        v2u kvm;
        if (wave < 4) {
            f32x4 aq = {0.f, 0.f, 0.f, 0.f}, ak = {0.f, 0.f, 0.f, 0.f};
#pragma unroll
            for (int ks = 0; ks < 14; ++ks) {
                bf16x8 mf = *(const bf16x8*)(sMem + r16 * MS + ks * 32 + kg * 8);
                aq = MFMA16(wreg[ks], mf, aq);
                ak = MFMA16(wreg[14 + ks], mf, ak);
            }
            v2u vq = {pack2(aq[0], aq[1]), pack2(aq[2], aq[3])};
            v2u vk = {pack2(ak[0], ak[1]), pack2(ak[2], ak[3])};
            *(v2u*)(sQ + r16 * 72 + wave * 16 + 4 * kg) = vq;
            *(v2u*)(sKm + r16 * 72 + wave * 16 + 4 * kg) = vk;
            kvm = vk;
        } else {
            f32x4 av = {0.f, 0.f, 0.f, 0.f};
#pragma unroll
            for (int ks = 0; ks < 14; ++ks) {
                bf16x8 mf = *(const bf16x8*)(sMem + r16 * MS + ks * 32 + kg * 8);
                av = MFMA16(mf, wreg[ks], av);
            }
            v2u vv = {san(pack2(av[0], av[1])), san(pack2(av[2], av[3]))};
#pragma unroll
            for (int c = 0; c < GG; ++c) BST64(vv, rX, vAv, sA0 + c * GG * XA_UNITS * 16);
            kvm = vv;
        }
        __syncthreads();
        STAMP(1)
        if (wave < 5) {
            f32x4 s = {0.f, 0.f, 0.f, 0.f};
#pragma unroll
            for (int ks = 0; ks < 2; ++ks) {
                bf16x8 qf = *(const bf16x8*)(sQ + r16 * 72 + ks * 32 + kg * 8);
                bf16x8 kf = (kxc ? act1 : act0) ? kxf[ks] : zero8;
                if (wave == 0) { v4u kl = *(const v4u*)(sKm + r16 * 72 + ks * 32 + kg * 8); asm volatile("" : "+v"(kl.x), "+v"(kl.y), "+v"(kl.z), "+v"(kl.w)); kf = __builtin_bit_cast(bf16x8, kl); }
                s = MFMA16(kf, qf, s);
            }
            v4u v = san4(__builtin_bit_cast(v4u, s));
            if (svalid) {
#pragma unroll
                for (int c = 0; c < GG; ++c) BST(v, rX, vT, sA0 + c * GG * XA_UNITS * 16);
            }
        }
        asm volatile("" ::: "memory");
        {
            const unsigned t0o = (unsigned)(tc0 * 16 + h), t1o = (unsigned)(tc1 * 16 + h);
            if (wave < 4) {
                if (cr ? act1 : act0) BST64(kvm, rD, vKV + (cr ? t1o : t0o) * 4096u, (unsigned)(g * 8 * 8 * 16));
            } else {
                if (ck ? act1 : act0) BST64(kvm, rD, vKV + (ck ? tc1 : tc0) * (unsigned)(NH_ * 16), (unsigned)((h * DH_ + g * 64) * 16));
            }
        }
        if (doC) { BST(sent4, rX, vT, sC); BST(sent4, rX, vT, sC + 448 * 16); }
        const int n0 = T + 1 - start0, n1 = T + 1 - start1;
        const bool need0 = n0 >= 0 && n0 < NSEG_ && (n0 & 3) == 0, need1 = n1 >= 0 && n1 < NSEG_ && (n1 & 3) == 0;
        const unsigned fq0 = flag_load(ws, FL_QKV + (0 * 16 + ((n0 >> 2) & 15)) * 16 + h), fq1 = flag_load(ws, FL_QKV + (1 * 16 + ((n1 >> 2) & 15)) * 16 + h);
        STAMP(2)
        const unsigned sAp = (unsigned)((sl * NCH + wg) * GG * XA_UNITS * 16);
        if (wave < 5) {
            if (svalid) {
                v4u v[GG];
                int sp = 0;
                do {
                    asm volatile("" ::: "memory");
#pragma unroll
                    for (int p = 0; p < GG; ++p) v[p] = BLD(rX, vT, sAp + p * XA_UNITS * 16);
                    if (++sp == SPIN_CAP) { __hip_atomic_store(errw, 2u, __ATOMIC_RELAXED, __HIP_MEMORY_SCOPE_AGENT); break; }
                } while (!(okv(v[0]) && okv(v[1]) && okv(v[2]) && okv(v[3])));
                f32x4 s = __builtin_bit_cast(f32x4, v[0]);
#pragma unroll
                for (int p = 1; p < GG; ++p) s += __builtin_bit_cast(f32x4, v[p]);
                const int col = (wave == 0) ? (kg & 1) * 4 : 8 + ((wave - 1) & 1) * 16 + kg * 4;
                *(f32x4*)(sS + r16 * SSR + col) = s;
            }
        } else {
            v4u v0, v1, v2;
            int sp = 0;
            do {
                asm volatile("" ::: "memory");
                v0 = BLD(rX, vP0, sAp); v1 = BLD(rX, vP1, sAp); v2 = BLD(rX, vP2, sAp);
                if (++sp == SPIN_CAP) { __hip_atomic_store(errw, 3u, __ATOMIC_RELAXED, __HIP_MEMORY_SCOPE_AGENT); break; }
            } while (!(okv(v0) && okv(v1) && okv(v2)));
            { const int x = vu; *(v4u*)(sVT + (x >> 1) * 24 + (x & 1) * 8) = v0; }
            { const int x = vu + 192; *(v4u*)(sVT + (x >> 1) * 24 + (x & 1) * 8) = v1; }
            if (v3) { const int x = vu + 384; *(v4u*)(sVT + (x >> 1) * 24 + (x & 1) * 8) = v2; }
        }
        __syncthreads();
        STAMP(3)
        {
            const int row = 2 * wave + (lane >> 5), j = lane & 31;
            const float xr = sS[row * SSR + 8 + j];
            const float mr = sS[row * SSR + (j & 7)];
            const float x = rb(xr) * 0.0625f;
            const float m = j < 8 ? rb(mr) * 0.0625f : -3.0e38f;
            float mx = fmaxf(x, m);
            mx = rowmax16(mx); mx = xmax16(mx);
            const float ex = __expf(x - mx);
            const float em = j < 8 ? __expf(m - mx) : 0.f;
            float sum = ex + em;
            sum = rowsum16(sum); sum = xsum16(sum);
            const float inv = 1.0f / sum;
            sP[row * 48 + 8 + j] = f2bf(ex * inv);
            if (j < 8) sP[row * 48 + j] = f2bf(em * inv);
        }
        __syncthreads();
        {
            const bool hasm = kg == cr;
            const bf16x8 px = *(const bf16x8*)(sP + r16 * 48 + 8 + kg * 8);
            v4u pml = *(const v4u*)(sP + r16 * 48);
            asm volatile("" : "+v"(pml.x), "+v"(pml.y), "+v"(pml.z), "+v"(pml.w));
            const bf16x8 pm = hasm ? __builtin_bit_cast(bf16x8, pml) : zero8;
            const bf16x8 px0 = cr == 0 ? px : zero8, px1 = cr == 1 ? px : zero8;
#pragma unroll
            for (int tl = 0; tl < 2; ++tl) {
                const int et = wave * 2 + tl;
                bf16x8 vm = zero8;
                if (kg < 2) vm = *(const bf16x8*)(sVT + (et * 16 + r16) * 24 + kg * 8);
                f32x4 o = {0.f, 0.f, 0.f, 0.f};
                o = MFMA16(vm, pm, o);
                o = MFMA16(act0 ? vxf[0][tl] : zero8, px0, o);
                o = MFMA16(act1 ? vxf[1][tl] : zero8, px1, o);
                v2u v = {pack2(o[0], o[1]), pack2(o[2], o[3])};
                *(v2u*)(sO + r16 * 264 + et * 16 + 4 * kg) = v;
            }
        }
        __syncthreads();
        STAMP(4)
        const bool more = T + 1 < nticks;
        if (more && wave >= 4) {
            const int w4 = wave - 4;
            const unsigned sB = (unsigned)((((sl * NCH + g) * 16 + h) * 32 + w4 * 8) * 16);
#pragma unroll
            for (int tl = 0; tl < 2; ++tl) {
                const int dt = 2 * w4 + tl;
                if (dt < 7) {
                    f32x4 a = {0.f, 0.f, 0.f, 0.f};
#pragma unroll
                    for (int ks = 0; ks < 8; ++ks) {
                        bf16x8 of = *(const bf16x8*)(sO + r16 * 264 + ks * 32 + kg * 8);
                        a = MFMA16(wreg[14 + tl * 8 + ks], of, a);
                    }
                    v4u v = san4(__builtin_bit_cast(v4u, a));
                    BST(v, rX, vBs, sB + tl * 64);
                }
            }
        }
        asm volatile("" ::: "memory");
        if (wave < 5) {
            if (svalid) {
#pragma unroll
                for (int q = 0; q < GG; ++q) BST(sent4, rX, vT, sAp + q * XA_UNITS * 16);
            }
        } else {
            BST(sent4, rX, vP0, sAp); BST(sent4, rX, vP1, sAp);
            if (v3) BST(sent4, rX, vP2, sAp);
        }
        STAMP(5)
        {
            const unsigned sBp = (unsigned)((sl * NCH + wg) * 16 * 32 * 16);
            const bool doB = more && wave < 7;
            v4u v = zero4;
            asm volatile("" ::: "memory");
            if (doB) v = BLD(rX, vBp, sBp);
            asm volatile("" ::: "memory");
            if ((need0 && fq0 < 6u) || (need1 && fq1 < 6u)) wait_qkv(T + 1);
            load_x(T + 1);
            asm volatile("" ::: "memory");
            if (doB) {
                int sp = 0;
                while (!okv(v)) {
                    asm volatile("" ::: "memory");
                    v = BLD(rX, vBp, sBp);
                    if (++sp == SPIN_CAP) { __hip_atomic_store(errw, 4u, __ATOMIC_RELAXED, __HIP_MEMORY_SCOPE_AGENT); break; }
                }
                float s0 = rowsum16(__uint_as_float(v.x)), s1 = rowsum16(__uint_as_float(v.y)), s2 = rowsum16(__uint_as_float(v.z)), s3 = rowsum16(__uint_as_float(v.w));
                v2u o = {san(pack2(s0, s1)), san(pack2(s2, s3))};
                if (r16 == 0) *(v2u*)(sM + (wave * 4 + kg) * 4) = o;
            } else {
                __builtin_amdgcn_s_waitcnt(0x0F70);
            }
            __syncthreads();
            STAMP(6)
            if (more) {
                const unsigned sP = (unsigned)((sn * NCH * 896 + h * 56 + g * 14) * 16);
                BST(*(const v4u*)(sM + pk0 * 8), rX, vPub0, sP);
                if (pub1) BST(*(const v4u*)(sM + pk1 * 8), rX, vPub1, sP);
            }
            if (tid == 0) {
                if (act0) flag_add(ws, FL_MEM + (0 * 16 + (tc0 >> 2)) * 16 + h);
                if (act1) flag_add(ws, FL_MEM + (1 * 16 + (tc1 >> 2)) * 16 + h);
            }
        }
        STAMP(7)
    }
}

constexpr int LAG = 10;
constexpr int DGRP = 3;
constexpr int NT_I = 16 * 96;
constexpr int NT_G = 16 + 16 + 96 + 16 + 16;
constexpr int NT_ALL = NT_I + (16 + DGRP) * NT_G;

__global__ void __launch_bounds__(512, 2) k_mega(const u16* __restrict__ mem0, char* __restrict__ ws_, u16* __restrict__ out_) {
    __shared__ __attribute__((aligned(16))) char smem[LDS_BYTES];
    __shared__ int s_ticket;
    if (blockIdx.x < NCH) { chain_role(mem0, ws_, smem, LAG, NSEG_ + LAG); return; }
    for (;;) {
        __syncthreads();
        if (threadIdx.x == 0) s_ticket = (int)__hip_atomic_fetch_add(flag_ptr(ws_, FL_TICKET), 1u, __ATOMIC_RELAXED, __HIP_MEMORY_SCOPE_AGENT);
        __syncthreads();
        int tk = __builtin_amdgcn_readfirstlane(s_ticket);
        if (tk >= NT_ALL) break;
        char* ws = ws_; asm volatile("" : "+s"(ws));
#define TRC nullptr
        u16* out = out_; asm volatile("" : "+s"(out));
        int tid = threadIdx.x; asm volatile("" : "+v"(tid));
        if (tk < NT_I) {
            const int G = tk / 96, j = tk % 96, nb = (j + 32) % 96;
            TR(1) task_qkv(ws, smem, 0, G, nb, tid); TR(2)
            signal_flag(ws, FL_QKV + (0 * 16 + G) * 16 + ((nb >> 1) & 15));
            continue;
        }
        tk -= NT_I;
        const int G = tk / NT_G; int j = tk % NT_G;
        if (j < 16) {
            if (G < 16) {
                wait_flag(ws, FL_QKV + (0 * 16 + G) * 16 + j, 6u);
                wait_flag(ws, FL_MEM + (0 * 16 + G) * 16 + j, 16u);
                TR(1) task_att(ws, smem, 0, G, j, tid, TRC); TR(2)
                signal_flag(ws, FL_ATT + 0 * 16 + G);
            }
            continue;
        }
        j -= 16;
        if (j < 16) {
            if (G < 16) {
                wait_flag(ws, FL_ATT + 0 * 16 + G, 16u);
                TR(1) task_red(ws, out, 0, G * 16 + j, tid); TR(2)
                signal_flag(ws, FL_RED + 0 * 16 + G);
            }
            continue;
        }
        j -= 16;
        if (j < 96) {
            if (G < 16) {
                const int nb = (j + 32) % 96;
                wait_flag(ws, FL_RED + 0 * 16 + G, 16u);
                TR(1) task_qkv(ws, smem, 1, G, nb, tid); TR(2)
                signal_flag(ws, FL_QKV + (1 * 16 + G) * 16 + ((nb >> 1) & 15));
            }
            continue;
        }
        j -= 96;
        const int G1 = G - DGRP;
        if (G1 < 0) continue;
        if (j < 16) {
            wait_flag(ws, FL_QKV + (1 * 16 + G1) * 16 + j, 6u);
            wait_flag(ws, FL_MEM + (1 * 16 + G1) * 16 + j, 16u);
            TR(1) task_att(ws, smem, 1, G1, j, tid, TRC); TR(2)
            signal_flag(ws, FL_ATT + 1 * 16 + G1);
            continue;
        }
        j -= 16;
        wait_flag(ws, FL_ATT + 1 * 16 + G1, 16u);
        TR(1) task_red(ws, out, 1, G1 * 16 + j, tid); TR(2)
    }
}

extern "C" void kernel_launch(void* const* d_in, const int* in_sizes, int n_in, void* d_out, int out_size, void* d_ws, size_t ws_size, hipStream_t stream) {
    const u16* A_q = (const u16*)d_in[0];
    const u16* A_k = (const u16*)d_in[1];
    const u16* A_v = (const u16*)d_in[2];
    const u16* A_shk = (const u16*)d_in[3];
    const u16* A_ssmk = (const u16*)d_in[4];
    const u16* A_ssmD = (const u16*)d_in[5];
    const u16* B_mem0 = (const u16*)d_in[7];
    const u16* B_Wq = (const u16*)d_in[8];
    const u16* B_Wk = (const u16*)d_in[9];
    const u16* B_Wv = (const u16*)d_in[10];
    const u16* B_Wo = (const u16*)d_in[11];
    char* ws = (char*)d_ws;
    u16* out = (u16*)d_out;

    k_pre1<<<NB_PRE1, 256, 0, stream>>>(A_k, A_v, A_shk, ws);
    k_pre2<<<NB_PRE2, 256, 0, stream>>>(B_Wq, B_Wk, B_Wv, B_Wo, A_ssmk, A_ssmD, ws);
    k_stage1<<<256, 512, 0, stream>>>(A_q, ws);
    k_mega<<<256, 512, 0, stream>>>(B_mem0, ws, out);
}
```

```cpp
#include <hip/hip_runtime.h>
#include <hip/hip_bf16.h>
#include <stdint.h>
#define DW0 80
#define WAUX 16
#define FOLD_MFIRST 1
#define SAUX 16
#define FASTSUM 1
#define WSL 12

constexpr int S_ = 2048, D_ = 448, H_ = 16, DH_ = 256, NH_ = 4096, SEG_ = 32, NSEG_ = 64, MM_ = 8;

typedef unsigned short u16;
typedef __attribute__((ext_vector_type(8))) short bf16x8;
typedef __attribute__((ext_vector_type(4))) float f32x4;
typedef __attribute__((ext_vector_type(16))) float f32x16;
typedef unsigned v4u __attribute__((ext_vector_type(4)));
typedef unsigned v2u __attribute__((ext_vector_type(2)));

#define DI __device__ __forceinline__
#define MFMA32(a, b, c) __builtin_amdgcn_mfma_f32_32x32x16_bf16((a), (b), (c), 0, 0, 0)
#define MFMA16(a, b, c) __builtin_amdgcn_mfma_f32_16x16x32_bf16((a), (b), (c), 0, 0, 0)

DI float bf2f(u16 v) { return __uint_as_float(((unsigned)v) << 16); }
typedef __attribute__((ext_vector_type(2))) __bf16 bf16x2_t;
typedef __attribute__((ext_vector_type(2))) float f32x2;
DI unsigned pack2(float a, float b) { f32x2 f = {a, b}; return __builtin_bit_cast(unsigned, __builtin_convertvector(f, bf16x2_t)); }
DI u16 f2bf(float f) { return (u16)(pack2(f, 0.f) & 0xffffu); }
DI float rb(float f) { return __uint_as_float(pack2(f, 0.f) << 16); }

constexpr size_t al(size_t x) { return (x + 255) & ~(size_t)255; }
constexpr int NPART = 3;
constexpr int NW_ = 16 * NPART * 448;
constexpr size_t OFF_WAB   = 0;
constexpr size_t OFF_KVT   = OFF_WAB + al((size_t)NW_ * 448 * 2);
constexpr size_t OFF_YT    = OFF_KVT + al((size_t)448 * 2048 * 2);
constexpr size_t OFF_Y     = OFF_YT + al((size_t)448 * 2048 * 2);
constexpr size_t OFF_X1    = OFF_Y + al((size_t)448 * 2048 * 2);
constexpr size_t OFF_X3    = OFF_X1 + al((size_t)2048 * 448 * 2);
constexpr size_t OFF_HB    = OFF_X3 + al((size_t)2048 * 448 * 2);
constexpr unsigned RU_BLK  = 28672;
constexpr size_t OFF_R     = OFF_HB + al((size_t)2 * 2048 * 448 * 2);
constexpr size_t OFF_U     = OFF_R + al((size_t)2 * 64 * 16 * RU_BLK);
constexpr size_t OFF_K     = OFF_U + al((size_t)2 * 64 * 16 * RU_BLK);
constexpr size_t OFF_MEMH  = OFF_K + al((size_t)2 * 64 * 16 * RU_BLK);
constexpr size_t OFF_WM    = OFF_MEMH + al((size_t)2 * 64 * 7168);
constexpr size_t OFF_PART  = OFF_WM + al((size_t)2 * 64 * 16 * 7168);
#define HSE 0
#define FUE 1
#define NCC 3
#define NCP 1
#define NSP 3
constexpr int RING = 4, NS_ = 16, NV_ = 28, NP_ = 84;
constexpr int NPR = (14 + NSP - 1) / NSP;
constexpr unsigned XC_SZ = 28 * 32 * 16;
constexpr unsigned XP_E = 0, XP_ST = 16384, XP_SM = 18432;
constexpr unsigned XP_SZ = XP_SM + NSP * 8192;
constexpr size_t OFF_XC    = OFF_PART + al((size_t)8 * 2048 * 448 * 4);
constexpr size_t SZ_XC     = (size_t)RING * NCC * XC_SZ;
constexpr size_t OFF_XP    = OFF_XC + al(SZ_XC);
constexpr size_t SZ_XP     = (size_t)RING * NCP * XP_SZ;
constexpr size_t OFF_FL    = OFF_XP + al(SZ_XP);
constexpr unsigned FL_ERR = 0;
#define PFS 4
constexpr unsigned PFB = 128 * PFS;
constexpr unsigned FL_PROJ = 256;
constexpr unsigned FL_ATT = FL_PROJ + 128 * PFB;
constexpr unsigned FL_RED = FL_ATT + 128 * 128;
constexpr unsigned FL_MEM = FL_RED + 64 * 128;
constexpr size_t SZ_FL     = (size_t)FL_MEM + 128 * 1024;
constexpr size_t WS_END    = OFF_FL + al(SZ_FL);
static_assert(WS_END + 256 * 640 * 4 <= (size_t)256 * 1024 * 1024, "workspace");

template <int LD>
DI void tile_load(const u16* __restrict__ src, int C, int r0, int c0, unsigned (*T)[LD]) {
    const int tid = threadIdx.x, rp = tid >> 3, cq = tid & 7;
    const v4u a = *(const v4u*)(src + (size_t)(r0 + 2 * rp) * C + c0 + cq * 8);
    const v4u b = *(const v4u*)(src + (size_t)(r0 + 2 * rp + 1) * C + c0 + cq * 8);
    const unsigned wa[4] = {a.x, a.y, a.z, a.w}, wb[4] = {b.x, b.y, b.z, b.w};
#pragma unroll
    for (int j = 0; j < 4; ++j) {
        T[cq * 8 + 2 * j][rp] = (wa[j] & 0xffffu) | (wb[j] << 16);
        T[cq * 8 + 2 * j + 1][rp] = (wa[j] >> 16) | (wb[j] & 0xffff0000u);
    }
}
DI void tile_store(unsigned (*T)[33], u16* __restrict__ dst, int ldd, int drow0, int dcol0) {
    const int tid = threadIdx.x;
#pragma unroll
    for (int it = 0; it < 2; ++it) {
        const int i = tid + 256 * it, c = i >> 3, rq = i & 7;
        v4u v = {T[c][rq * 4], T[c][rq * 4 + 1], T[c][rq * 4 + 2], T[c][rq * 4 + 3]};
        *(v4u*)(dst + (size_t)(drow0 + c) * ldd + dcol0 + rq * 8) = v;
    }
}

constexpr int HP = 2144;
constexpr int NB_TR = 2 * 16 * 7;
constexpr size_t FILL_BYTES = OFF_FL - OFF_XC;
constexpr int NB_FILL = (int)((FILL_BYTES + 65535) / 65536);
constexpr int NB_ZERO = (int)((SZ_FL + 65535) / 65536);
constexpr int NB_PRE = NB_TR + NB_FILL + NB_ZERO;
static_assert(NB_PRE <= 256, "pre grid");
DI size_t wab_off(int hp, int n, int k) { return ((size_t)(hp * 28 + (n >> 4)) * 56 + (k >> 3)) * 128 + (n & 15) * 8 + (k & 7); }
constexpr int CONV_LDS = 2 * HP * 2 + 96 * 40 * 2;
constexpr int FOLD_BUF = 512 * 72 * 2;
constexpr int PRE_LDS = 2 * FOLD_BUF + 64 * 33 * 4;

__global__ void __launch_bounds__(512) k_pre1(const u16* __restrict__ kin, const u16* __restrict__ vin, const u16* __restrict__ shk,
                                               const u16* __restrict__ Wq, const u16* __restrict__ Wk, const u16* __restrict__ Wv, const u16* __restrict__ Wo, char* __restrict__ ws) {
    __shared__ __attribute__((aligned(16))) char smem[PRE_LDS];
    int b = blockIdx.x;
    const int tid = threadIdx.x;
    if (b >= NB_TR) {
        b -= NB_TR;
        const bool fill = b < NB_FILL;
        if (!fill) b -= NB_FILL;
        char* p = ws + (fill ? OFF_XC : OFF_FL) + (size_t)b * 65536;
        const size_t lim = fill ? FILL_BYTES : SZ_FL;
        const unsigned w = fill ? 0xFFFFFFFFu : 0u;
        const v4u s = {w, w, w, w};
#pragma unroll
        for (int it = 0; it < 8; ++it) {
            const size_t o = (size_t)b * 65536 + (size_t)(it * 512 + tid) * 16;
            if (o < lim) *(v4u*)(p + (size_t)(it * 512 + tid) * 16) = s;
        }
        return;
    }
    const int lane = tid & 63;
    const int wave = __builtin_amdgcn_readfirstlane(tid >> 6);
#define FST(i)
    FST(0)
    const int t0 = (b / 7) * 64, d0 = (b % 7) * 64;
    const int cq = tid & 7, tp = (tid >> 3) & 31, t = t0 + 2 * tp, dc = d0 + 8 * cq;
    v4u kk[5], vv[2], sw4[4];
    if (wave < 4) {
#pragma unroll
        for (int j = 0; j < 5; ++j) { const int ts = t - 3 + j; kk[j] = *(const v4u*)(kin + (size_t)(ts >= 0 ? ts : 0) * D_ + dc); }
#pragma unroll
        for (int j = 0; j < 2; ++j) vv[j] = *(const v4u*)(vin + (size_t)(t + j) * D_ + dc);
#pragma unroll
        for (int j = 0; j < 4; ++j) sw4[j] = *(const v4u*)(shk + dc * 4 + 8 * j);
    }
    const int gidx = (b & 7) + 8 * ((b >> 3) / 7);
    const int part = gidx >> 4, h = gidx & 15, c0 = ((b >> 3) % 7) * 64;
    const u16* W1 = (part ? Wv : Wq) + h * DH_;
    v4u pa[2][7], pb[2];
    auto issue = [&](int ch) {
#pragma unroll
        for (int i = 0; i < 7; ++i) { const int u = tid + 512 * i; pa[ch & 1][i] = *(const v4u*)(W1 + (size_t)(u >> 3) * NH_ + ch * 64 + (u & 7) * 8); }
        if (!part) pb[ch & 1] = *(const v4u*)(Wk + (size_t)(c0 + (tid >> 3)) * NH_ + h * DH_ + ch * 64 + (tid & 7) * 8);
        else pb[ch & 1] = *(const v4u*)(Wo + (size_t)(h * DH_ + ch * 64 + (tid >> 3)) * D_ + c0 + (tid & 7) * 8);
        __builtin_amdgcn_sched_barrier(0);
    };
    issue(0); issue(1);
    {
        unsigned (*T)[33] = (unsigned (*)[33])(smem + 2 * FOLD_BUF);
        if (wave < 4) {
#pragma unroll
            for (int j = 0; j < 3; ++j) if (t - 3 + j < 0) kk[j] = (v4u){0u, 0u, 0u, 0u};
#pragma unroll
            for (int c = 0; c < 8; ++c) {
                const unsigned sa = (c & 1) ? sw4[c >> 1][2] : sw4[c >> 1][0], sb = (c & 1) ? sw4[c >> 1][3] : sw4[c >> 1][1];
                const float sw[4] = {__uint_as_float(sa << 16), __uint_as_float(sa & 0xffff0000u), __uint_as_float(sb << 16), __uint_as_float(sb & 0xffff0000u)};
                float kc[5], vc[2];
#pragma unroll
                for (int j = 0; j < 5; ++j) { const unsigned w = kk[j][c >> 1]; kc[j] = __uint_as_float((c & 1) ? (w & 0xffff0000u) : (w << 16)); }
#pragma unroll
                for (int j = 0; j < 2; ++j) { const unsigned w = vv[j][c >> 1]; vc[j] = __uint_as_float((c & 1) ? (w & 0xffff0000u) : (w << 16)); }
                float kv[2];
#pragma unroll
                for (int j = 0; j < 2; ++j) {
                    float acc = 0.f;
#pragma unroll
                    for (int jj = 0; jj < 4; ++jj) { const float pq = rb(kc[j + jj] * sw[3 - jj]); acc = rb(acc + pq); }
                    kv[j] = rb(acc * vc[j]);
                }
                T[8 * cq + c][(tp & 16) + 15 - (tp & 15)] = pack2(kv[1], kv[0]);
            }
        }
        __syncthreads();
        if (wave < 4) tile_store(T, (u16*)(ws + OFF_KVT), S_, d0, t0);
    }
    FST(1)
    {
        const int r = lane & 31, hh = lane >> 5;
        const int pr = (r & 19) | ((r & 4) << 1) | ((r & 8) >> 1);
        auto stage = [&](int ch) {
            u16* sA = (u16*)(smem + (ch & 1) * FOLD_BUF);
            u16* sB = sA + 448 * 72;
#pragma unroll
            for (int i = 0; i < 7; ++i) { const int u = tid + 512 * i; *(v4u*)(sA + (u >> 3) * 72 + (u & 7) * 8) = pa[ch & 1][i]; }
            if (!part) *(v4u*)(sB + (tid >> 3) * 72 + (tid & 7) * 8) = pb[ch & 1];
            else {
                const unsigned w[4] = {pb[ch & 1].x, pb[ch & 1].y, pb[ch & 1].z, pb[ch & 1].w};
#pragma unroll
                for (int j = 0; j < 4; ++j) {
                    sB[((tid & 7) * 8 + 2 * j) * 72 + (tid >> 3)] = (u16)(w[j] & 0xffffu);
                    sB[((tid & 7) * 8 + 2 * j + 1) * 72 + (tid >> 3)] = (u16)(w[j] >> 16);
                }
            }
        };
        f32x16 acc[2][2];
#pragma unroll
        for (int i = 0; i < 16; ++i) { acc[0][0][i] = 0.f; acc[0][1][i] = 0.f; acc[1][0][i] = 0.f; acc[1][1][i] = 0.f; }
        stage(0);
        __builtin_amdgcn_sched_barrier(0);
        issue(2);
        __syncthreads();
#pragma unroll
        for (int ch = 0; ch < 4; ++ch) {
            FST(2 + ch)
            const u16* sA = (const u16*)(smem + (ch & 1) * FOLD_BUF);
            const u16* sB = sA + 448 * 72;
            if (wave < 7) {
#pragma unroll
                for (int ks = 0; ks < 4; ++ks) {
                    bf16x8 af[2], bfr[2];
#pragma unroll
                    for (int x = 0; x < 2; ++x) {
                        af[x] = *(const bf16x8*)(sA + (wave * 64 + x * 32 + pr) * 72 + ks * 16 + hh * 8);
                        bfr[x] = *(const bf16x8*)(sB + (x * 32 + r) * 72 + ks * 16 + hh * 8);
                    }
#pragma unroll
                    for (int x = 0; x < 2; ++x)
#pragma unroll
                        for (int y = 0; y < 2; ++y) acc[x][y] = MFMA32(af[x], bfr[y], acc[x][y]);
                }
            }
            __builtin_amdgcn_sched_barrier(0);
            if (ch < 3) { stage(ch + 1); __builtin_amdgcn_sched_barrier(0); if (ch < 1) issue(ch + 3); }
            __syncthreads();
        }
        FST(6)
        auto rWo = __builtin_amdgcn_make_buffer_rsrc(ws + OFF_WAB, 0, (unsigned)((size_t)NW_ * 448 * 2), 0x00020000);
        u16* T2 = (u16*)smem;
        if (wave < 7) {
#pragma unroll
            for (int x = 0; x < 2; ++x)
#pragma unroll
                for (int y = 0; y < 2; ++y) {
#pragma unroll
                    for (int a = 0; a < 2; ++a) {
                        const f32x16& c = acc[x][y];
                        const int kd = wave * 64 + x * 32 + 16 * a + 8 * hh;
                        v4u v = {pack2(c[8 * a], c[8 * a + 1]), pack2(c[8 * a + 2], c[8 * a + 3]), pack2(c[8 * a + 4], c[8 * a + 5]), pack2(c[8 * a + 6], c[8 * a + 7])};
                        __builtin_amdgcn_raw_buffer_store_b128(v, rWo, (unsigned)wab_off(h * NPART + part, c0 + y * 32 + r, kd) * 2u, 0, WAUX);
                        if (!part) {
                            const bool odd = r & 1;
                            u16* dt = T2 + (kd + (odd ? 1 : 0)) * 72 + y * 32 + (r & ~1);
#pragma unroll
                            for (int j = 0; j < 8; j += 2) {
                                const float snd = odd ? c[8 * a + j] : c[8 * a + j + 1];
                                const float rcv = __uint_as_float(__builtin_amdgcn_update_dpp(0u, __float_as_uint(snd), 0xB1, 0xF, 0xF, false));
                                *(unsigned*)(dt + j * 72) = odd ? pack2(rcv, c[8 * a + j + 1]) : pack2(c[8 * a + j], rcv);
                            }
                        }
                    }
                }
        }
        if (!part) {
            __syncthreads();
#pragma unroll
            for (int i = 0; i < 7; ++i) {
                const int u = tid + 512 * i, dd = (u >> 7) * 16 + (u & 15), kq = (u >> 4) & 7;
                __builtin_amdgcn_raw_buffer_store_b128(*(const v4u*)(T2 + dd * 72 + kq * 8), rWo, (unsigned)wab_off(h * NPART + 2, dd, c0 + 8 * kq) * 2u, 0, WAUX);
            }
        }
        FST(7)
    }
}

__global__ void __launch_bounds__(256) k_pre2(const u16* __restrict__ ssmk, const u16* __restrict__ ssmD, char* __restrict__ ws) {
    __shared__ __attribute__((aligned(16))) char smem[CONV_LDS + 4 * 8192];
    const int d = blockIdx.x;
    const int tid = threadIdx.x, lane = tid & 63;
    const int wq = __builtin_amdgcn_readfirstlane(tid >> 6);
    u16* hE = (u16*)smem; u16* hO = hE + HP; u16* kvs = hO + HP;
    f32x4* prt = (f32x4*)(smem + CONV_LDS);
    const u16* hrow = ssmk + (size_t)d * S_;
    const v4u hv = *(const v4u*)(hrow + 8 * tid);
    const unsigned hn_ = (unsigned)hrow[tid < 255 ? 8 * tid + 8 : 0];
    const v4u kvv = *(const v4u*)((const u16*)(ws + OFF_KVT) + (size_t)d * S_ + 8 * tid);
    const u16 Dh = ssmD[d];
    const unsigned hn = tid < 255 ? hn_ : 0u;
    if (tid < 16) { ((unsigned*)hE)[tid] = 0u; }
    if (tid >= 16 && tid < 31) ((unsigned*)hO)[tid - 16] = 0u;
    for (int i = tid; i < 640; i += 256) ((unsigned*)kvs)[i] = 0u;
    *(v4u*)(hE + 32 + 8 * tid) = hv;
    v4u ho = {(hv.x >> 16) | (hv.y << 16), (hv.y >> 16) | (hv.z << 16), (hv.z >> 16) | (hv.w << 16), (hv.w >> 16) | (hn << 16)};
    *(v4u*)(hO + 32 + 8 * tid) = ho;
    if (tid == 0) ((unsigned*)hO)[15] = hv.x << 16;
    *(v4u*)(kvs + (32 + (tid >> 2)) * 40 + 8 * (tid & 3)) = kvv;
    __syncthreads();
    const int m = lane & 15, q = lane >> 4;
    const unsigned* ap = (m & 1) ? (const unsigned*)(hE + m + 1 + 8 * q) : (const unsigned*)(hO + m + 8 * q);
    f32x4 acc[4][2];
#pragma unroll
    for (int g = 0; g < 4; ++g) { acc[g][0] = (f32x4){0.f, 0.f, 0.f, 0.f}; acc[g][1] = acc[g][0]; }
#pragma unroll 2
    for (int z = 0; z < 4; ++z) {
        const int dl0 = 4 * wq + z;
        bf16x8 bq[4];
#pragma unroll
        for (int g = 0; g < 4; ++g) bq[g] = *(const bf16x8*)(kvs + (32 + 16 * g + m - dl0) * 40 + 8 * q);
#pragma unroll
        for (int j = 0; j < 4; ++j) {
            const unsigned* a = ap + 16 * (dl0 + 16 * j);
            const v4u a0 = {a[0], a[1], a[2], a[3]}, a1 = {a[8], a[9], a[10], a[11]};
#pragma unroll
            for (int g = j; g < 4; ++g) {
                acc[g][0] = MFMA16(__builtin_bit_cast(bf16x8, a0), bq[g - j], acc[g][0]);
                acc[g][1] = MFMA16(__builtin_bit_cast(bf16x8, a1), bq[g - j], acc[g][1]);
            }
        }
    }
#pragma unroll
    for (int g = 0; g < 4; ++g) { prt[((wq * 4 + g) * 2) * 64 + lane] = acc[g][0]; prt[((wq * 4 + g) * 2 + 1) * 64 + lane] = acc[g][1]; }
    __syncthreads();
    const float Dd = bf2f(Dh);
    const int icol = 16 * wq + m;
    auto rY = __builtin_amdgcn_make_buffer_rsrc(ws + OFF_YT, 0, D_ * S_ * 2, 0x00020000);
#pragma unroll
    for (int mt = 0; mt < 2; ++mt) {
        f32x4 a = prt[((0 * 4 + wq) * 2 + mt) * 64 + lane];
#pragma unroll
        for (int w = 1; w < 4; ++w) a += prt[((w * 4 + wq) * 2 + mt) * 64 + lane];
        const v2u kr = *(const v2u*)(kvs + (32 + icol) * 40 + 28 - 16 * mt - 4 * q);
        const float k3 = __uint_as_float(kr.x << 16), k2 = __uint_as_float(kr.x & 0xffff0000u);
        const float k1 = __uint_as_float(kr.y << 16), k0 = __uint_as_float(kr.y & 0xffff0000u);
        const float y0 = rb(rb(a[0]) + rb(k0 * Dd)), y1 = rb(rb(a[1]) + rb(k1 * Dd));
        const float y2 = rb(rb(a[2]) + rb(k2 * Dd)), y3 = rb(rb(a[3]) + rb(k3 * Dd));
        v2u o = {pack2(y0, y1), pack2(y2, y3)};
        __builtin_amdgcn_raw_buffer_store_b64(o, rY, (unsigned)((d * S_ + 32 * icol + 16 * mt + 4 * q) * 2), 0, 16);
    }
}

template <int CTRL> DI float dpp_f0(float v) { return __uint_as_float(__builtin_amdgcn_update_dpp(0u, __float_as_uint(v), CTRL, 0xF, 0xF, false)); }
DI float wave_sum(float v) {
    v += dpp_f0<0x128>(v);
    v += dpp_f0<0x124>(v);
    v += dpp_f0<0x122>(v);
    v += dpp_f0<0x121>(v);
    { auto r = __builtin_amdgcn_permlane16_swap(__float_as_uint(v), __float_as_uint(v), false, false); v = __uint_as_float(r[0]) + __uint_as_float(r[1]); }
    { auto r = __builtin_amdgcn_permlane32_swap(__float_as_uint(v), __float_as_uint(v), false, false); v = __uint_as_float(r[0]) + __uint_as_float(r[1]); }
    return v;
}
DI void layernorm7(const float (&x)[7], float (&o)[7]) {
    float s = 0.f;
#pragma unroll
    for (int i = 0; i < 7; ++i) s += x[i];
    s = wave_sum(s);
    const float mean = s * (1.0f / 448.0f);
    float q = 0.f;
#pragma unroll
    for (int i = 0; i < 7; ++i) { float dlt = x[i] - mean; q += dlt * dlt; }
    q = wave_sum(q);
    const float m = rb(mean), v = rb(q * (1.0f / 448.0f));
    const float den = rb(sqrtf(rb(v + 1e-5f)));
#pragma unroll
    for (int i = 0; i < 7; ++i) o[i] = rb(rb(x[i] - m) / den);
}

DI void layernorm8(const float (&x)[8], float (&o)[8], bool on) {
    float s = 0.f;
#pragma unroll
    for (int i = 0; i < 8; ++i) s += x[i];
    s = wave_sum(s);
    const float mean = s * (1.0f / 448.0f);
    float q = 0.f;
#pragma unroll
    for (int i = 0; i < 8; ++i) { float dlt = x[i] - mean; q += on ? dlt * dlt : 0.f; }
    q = wave_sum(q);
    const float m = rb(mean), v = rb(q * (1.0f / 448.0f));
    const float den = rb(sqrtf(rb(v + 1e-5f)));
#pragma unroll
    for (int i = 0; i < 8; ++i) o[i] = on ? rb(rb(x[i] - m) / den) : 0.f;
}
DI void unpack8(v4u w, float (&x)[8]) {
    x[0] = __uint_as_float(w.x << 16); x[1] = __uint_as_float(w.x & 0xffff0000u); x[2] = __uint_as_float(w.y << 16); x[3] = __uint_as_float(w.y & 0xffff0000u);
    x[4] = __uint_as_float(w.z << 16); x[5] = __uint_as_float(w.z & 0xffff0000u); x[6] = __uint_as_float(w.w << 16); x[7] = __uint_as_float(w.w & 0xffff0000u);
}
DI v4u pack8(const float (&x)[8]) { v4u w = {pack2(x[0], x[1]), pack2(x[2], x[3]), pack2(x[4], x[5]), pack2(x[6], x[7])}; return w; }
__global__ void __launch_bounds__(512) k_stage1(const u16* __restrict__ x0, char* __restrict__ ws) {
    __shared__ __attribute__((aligned(16))) u16 yt[8][456];
    const int t0 = blockIdx.x * 8, tid = threadIdx.x, lane = tid & 63, wave = tid >> 6;
    const u16* yT = (const u16*)(ws + OFF_YT);
    const int t = t0 + wave;
    const bool on = lane < 56;
    const v4u xw = *(const v4u*)(x0 + (size_t)t * D_ + (on ? lane : 0) * 8);
    v4u w = {0u, 0u, 0u, 0u};
    if (tid < 448) w = *(const v4u*)(yT + (size_t)tid * S_ + t0);
    float x[8], ln[8], y[8];
    unpack8(xw, x);
#pragma unroll
    for (int j = 0; j < 8; ++j) x[j] = on ? x[j] : 0.f;
    layernorm8(x, ln, on);
    if (tid < 448) {
        yt[0][tid] = (u16)(w.x & 0xffff); yt[1][tid] = (u16)(w.x >> 16);
        yt[2][tid] = (u16)(w.y & 0xffff); yt[3][tid] = (u16)(w.y >> 16);
        yt[4][tid] = (u16)(w.z & 0xffff); yt[5][tid] = (u16)(w.z >> 16);
        yt[6][tid] = (u16)(w.w & 0xffff); yt[7][tid] = (u16)(w.w >> 16);
    }
    __syncthreads();
    const v4u yw = *(const v4u*)(&yt[wave][(on ? lane : 0) * 8]);
    unpack8(yw, y);
#pragma unroll
    for (int j = 0; j < 8; ++j) x[j] = on ? rb(x[j] + rb(ln[j] * y[j])) : 0.f;
    layernorm8(x, ln, on);
    auto rS = __builtin_amdgcn_make_buffer_rsrc(ws + OFF_Y, 0, (unsigned)(OFF_R - OFF_Y), 0x00020000);
    if (on) {
        const unsigned o = (unsigned)((t * D_ + lane * 8) * 2);
        __builtin_amdgcn_raw_buffer_store_b128(pack8(ln), rS, o, (unsigned)(OFF_HB - OFF_Y), SAUX);
        __builtin_amdgcn_raw_buffer_store_b128(pack8(x), rS, o, (unsigned)(OFF_X1 - OFF_Y), SAUX);
        __builtin_amdgcn_raw_buffer_store_b128(yw, rS, o, 0, SAUX);
    }
}

#define LAGV 6
constexpr int LAG = LAGV;
#define HPT0 2
#define HPT1 4
#define NA0V 3
#define NA1V 4
#define RW0 8
#define NR0V 3
#define PAHEAD (LAGV + 3)
constexpr int NA0 = NA0V, NA1 = NA1V, NR0 = NR0V;
constexpr int NG0 = 16 / HPT0, NG1 = 16 / HPT1;
constexpr int WG_SM = NS_, WG_V = WG_SM + NS_ * NSP, WG_P = WG_V + NV_, WG_A0 = WG_P + NP_, WG_A1 = WG_A0 + NG0 * NA0, WG_R0 = WG_A1 + NG1 * NA1, WG_R1 = WG_R0 + RW0 * NR0;
constexpr int NR1 = (256 - WG_R1) / 4;
static_assert(NR1 >= 2, "grid");

static_assert(NG0 <= 8 && NG1 <= 8, "consumers");
constexpr unsigned SENT = 0xFFFFFFFFu;
DI bool okv(v4u v) { return v.x != SENT && v.y != SENT && v.z != SENT && v.w != SENT; }
DI bool okv2(v2u v) { return v.x != SENT && v.y != SENT; }
DI unsigned san(unsigned w) { return w == SENT ? 0x7FC07FC0u : w; }
constexpr int SPIN_CAP = 1 << 16;
constexpr int WAIT_CAP = 1 << 16;
constexpr int MS = 456;
constexpr int SSR = 44;
constexpr int LDS_BYTES = 156 * 1024;

template <int CTRL> DI float dpp_f(float v) { return __uint_as_float(__builtin_amdgcn_update_dpp(0u, __float_as_uint(v), CTRL, 0xF, 0xF, false)); }
DI float rowsum16(float v) {
    v += dpp_f<0x128>(v);
    v += dpp_f<0x124>(v);
    v += dpp_f<0x122>(v);
    v += dpp_f<0x121>(v);
    return v;
}
DI float rowmax16(float v) {
    v = fmaxf(v, dpp_f<0x128>(v));
    v = fmaxf(v, dpp_f<0x124>(v));
    v = fmaxf(v, dpp_f<0x122>(v));
    v = fmaxf(v, dpp_f<0x121>(v));
    return v;
}

#define SLP(rt, ct) { const int n_ = (ct); for (int i_ = 0; i_ < n_; ++i_) __builtin_amdgcn_s_sleep(1); }
#define DLARG
#define DLPASS
#define DLPARAM
#define XMAP 0
#define MPF 3
#define KPF 3
#define DSC 40
#define DSM 90
#define DKL 0
#define HAUX 16
#define KAUX 16
#define DVC 4
#define DVP 0
#define PPIPE 0
#define TSINIT()
#define TSDUMP(ws)
#define TS(role, T, i)

#define BLD(r, vo, so) __builtin_amdgcn_raw_buffer_load_b128((r), (vo), (so), 16)
#define BLD64(r, vo, so) __builtin_amdgcn_raw_buffer_load_b64((r), (vo), (so), 16)
#define BST(v, r, vo, so) __builtin_amdgcn_raw_buffer_store_b128((v), (r), (vo), (so), 16)
#define BST64(v, r, vo, so) __builtin_amdgcn_raw_buffer_store_b64((v), (r), (vo), (so), 16)
#define VMWAIT() __builtin_amdgcn_s_waitcnt(0x0F70)

#define XPERM 0x76543210u
DI int xcd_of(int wg) { return (int)((XPERM >> (4 * (wg & 7))) & 7u); }
DI unsigned* flag_ptr(char* ws, unsigned off) { return (unsigned*)(ws + OFF_FL + off); }
DI unsigned flag_load(char* ws, unsigned off) { return __hip_atomic_load(flag_ptr(ws, off), __ATOMIC_RELAXED, __HIP_MEMORY_SCOPE_AGENT); }
DI void flag_set(char* ws, unsigned off) { __hip_atomic_store(flag_ptr(ws, off), 1u, __ATOMIC_RELAXED, __HIP_MEMORY_SCOPE_AGENT); }
DI void set_err(char* ws, unsigned code) { __hip_atomic_store(flag_ptr(ws, FL_ERR), code, __ATOMIC_RELAXED, __HIP_MEMORY_SCOPE_AGENT); }
template <int STR = 4>
DI bool flags_ready(char* ws, unsigned off, int n) {
    int lane = threadIdx.x & 63; asm volatile("" : "+v"(lane));
    unsigned v = lane < n ? flag_load(ws, off + lane * STR) : 1u;
    if (n > 64) { const unsigned v2 = lane + 64 < n ? flag_load(ws, off + 64 * STR + lane * STR) : 1u; v = v2 == 0u ? 0u : v; }
    return __builtin_amdgcn_ballot_w64(v == 0u) == 0ull;
}
template <int SL, int STR = 4>
DI void spin_flag(char* ws, unsigned off, int n) {
    int sp = 0;
    for (; sp < WAIT_CAP; ++sp) {
        if (flags_ready<STR>(ws, off, n)) break;
        if ((sp & 63) == 63 && flag_load(ws, FL_ERR) != 0u) break;
        __builtin_amdgcn_s_sleep(SL);
    }
    if (sp == WAIT_CAP) set_err(ws, 5u);
}
template <int STR = 4>
DI void wait_flag(char* ws, unsigned off, int n) {
    if (threadIdx.x < 64) spin_flag<WSL, STR>(ws, off, n);
    __syncthreads();
}
DI void signal_flag(char* ws, unsigned off) {
    VMWAIT();
    __syncthreads();
    if (threadIdx.x == 0) flag_set(ws, off);
}

#define CPF 1
static __device__ __attribute__((noinline, used)) void zz_end();
DI unsigned code_touch(unsigned long long pc, int sl, int ns) {
    unsigned acc = 0u;
    const unsigned long long b = pc & ~63ull;
    const long long n = (long long)((unsigned long long)(uintptr_t)&zz_end - b);
    if (n > 0 && n <= 262144) {
        const int per = (((int)n + ns - 1) / ns + 63) & ~63;
        const int hi = min((sl + 1) * per, (int)n - 16);
        for (int o = sl * per + (int)threadIdx.x * 16; o < hi; o += 512 * 16) { const v4u v = *(const v4u*)(b + o); acc ^= v.x; }
    }
    return acc;
}

DI void get_mem(const u16* __restrict__ mem0, char* ws, __amdgpu_buffer_rsrc_t rX, u16* sMem, unsigned sC, int T, int tid, bool act0, bool act1, int tc0, int tc1) {
    if (tid < 448) {
        v4u v0 = {0u, 0u, 0u, 0u}, v1 = {0u, 0u, 0u, 0u};
        if (T > 0) {
            int sp = 0;
            do {
                asm volatile("" ::: "memory");
                v0 = BLD(rX, tid * 16, sC);
                v1 = BLD(rX, tid * 16, sC + 448 * 16);
                __builtin_amdgcn_s_sleep(2);
                if (++sp == SPIN_CAP) { set_err(ws, 1u); break; }
            } while (!(okv(v0) && okv(v1)));
        }
        const int row = (tid >> 1) & 15, c0 = (tid >> 5) * 16 + (tid & 1) * 8, c1 = c0 + 14 * 16;
        const bool act = row < 8 ? act0 : act1;
        const int tc = row < 8 ? tc0 : tc1;
        if (!act) { v0 = (v4u){0u, 0u, 0u, 0u}; v1 = v0; }
        else if (tc == 0) {
            int t2 = tid; asm volatile("" : "+v"(t2));
            const int o2 = ((t2 >> 1) & 7) * D_ + (t2 >> 5) * 16 + (t2 & 1) * 8;
            v0 = *(const v4u*)(mem0 + o2); v1 = *(const v4u*)(mem0 + o2 + 14 * 16);
        }
        *(v4u*)(sMem + row * MS + c0) = v0;
        *(v4u*)(sMem + row * MS + c1) = v1;
    }
}

DI void get_frag(const u16* __restrict__ mem0, char* ws, __amdgpu_buffer_rsrc_t rX, unsigned sC, int T, int wave, int r16, int kg, bool act, int tc, v4u (&m)[2]) {
    m[0] = (v4u){0u, 0u, 0u, 0u}; m[1] = m[0];
    if (T > 0) {
        const unsigned vo = (unsigned)((4 * wave + (kg >> 1)) * 512 + r16 * 32 + (kg & 1) * 16);
        int sp = 0;
        do {
            asm volatile("" ::: "memory");
            m[0] = BLD(rX, vo, sC);
            m[1] = BLD(rX, vo + 1024, sC);
            __builtin_amdgcn_s_sleep(2);
            if (++sp == SPIN_CAP) { set_err(ws, 1u); break; }
        } while (!(okv(m[0]) && okv(m[1])));
    }
    if (!act) { m[0] = (v4u){0u, 0u, 0u, 0u}; m[1] = m[0]; }
    else if (tc == 0) {
        int t2 = r16; asm volatile("" : "+v"(t2));
        const int o2 = (t2 & 7) * D_ + 64 * wave + 8 * kg;
        m[0] = *(const v4u*)(mem0 + o2); m[1] = *(const v4u*)(mem0 + o2 + 32);
    }
}

constexpr int SXR = 36;
constexpr float SC_L2E = 0.0625f * 1.44269504088896340736f;
DI void sx_role(const u16* __restrict__ mem0, char* ws, char* smem, int h, int lag, int nticks DLPARAM) {
    asm volatile("" : "+s"(ws));
    float* sS = (float*)smem;
    const int tid = threadIdx.x, lane = tid & 63, r16 = lane & 15, kg = lane >> 4, cr = r16 >> 3;
    const int wave = __builtin_amdgcn_readfirstlane(tid >> 6);
    auto rX = __builtin_amdgcn_make_buffer_rsrc(ws + OFF_XC, 0, (unsigned)(OFF_FL - OFF_XC), 0x00020000);
    auto rK = __builtin_amdgcn_make_buffer_rsrc(ws + OFF_K, 0, (unsigned)(OFF_MEMH - OFF_K), 0x00020000);
    constexpr unsigned XP0 = (unsigned)(OFF_XP - OFF_XC);
    const v4u zero4 = {0u, 0u, 0u, 0u};
    const v2u sent2 = {SENT, SENT};
    v4u kx[2][2][2];
    const unsigned vK = (unsigned)(((8 * wave + kg) * 32 + r16) * 16);
    auto load_k = [&](int T) {
        const int t0 = T, t1 = T - lag;
        const bool a0 = t0 < NSEG_, a1 = t1 >= 0 && t1 < NSEG_;
        if (a0 && T > 0) spin_flag<4, PFS>(ws, FL_PROJ + t0 * PFB, NP_);
        if (a1) spin_flag<4, PFS>(ws, FL_PROJ + (64 + t1) * PFB, NP_);
#pragma unroll
        for (int c = 0; c < 2; ++c)
#pragma unroll
            for (int kt = 0; kt < 2; ++kt)
#pragma unroll
                for (int j = 0; j < 2; ++j) {
                    kx[c][kt][j] = zero4;
                    if (c ? a1 : a0) kx[c][kt][j] = BLD(rK, vK + (unsigned)((4 * j * 32 + 16 * kt) * 16), (unsigned)((c ? 64 + t1 : t0) * 16 + h) * RU_BLK);
                }
    };
    wait_flag<PFS>(ws, FL_PROJ, NP_);
    if (wave < 7) load_k(0);
    const int srow = tid >> 3, q8 = tid & 7;
    const unsigned vE = XP0 + XP_E + (unsigned)((h * 16 + srow) * 64 + q8 * 8);
    const unsigned vST = XP0 + XP_ST + (unsigned)((h * 16 + srow) * 8);
    for (int T = 0; T < nticks - 1; ++T) {
        const int sl = T & (RING - 1), spv = (T - 1) & (RING - 1);
        const int tc0 = T, tc1 = T - lag;
        const bool act0 = tc0 < NSEG_, act1 = tc1 >= 0 && tc1 < NSEG_;
        TS(0, T, 0)
        if (wave < 7) {
            SLP(dl[0], DSC)
            v4u m[2];
            get_frag(mem0, ws, rX, (unsigned)((sl * NCC + (xcd_of(blockIdx.x) % NCC)) * XC_SZ), T, wave, r16, kg, cr ? act1 : act0, cr ? tc1 : tc0, m);
            TS(0, T, 1)
            f32x4 d[2][2];
#pragma unroll
            for (int c = 0; c < 2; ++c)
#pragma unroll
                for (int kt = 0; kt < 2; ++kt) {
                    d[c][kt] = (f32x4){0.f, 0.f, 0.f, 0.f};
#pragma unroll
                    for (int j = 0; j < 2; ++j) d[c][kt] = MFMA16(__builtin_bit_cast(bf16x8, kx[c][kt][j]), __builtin_bit_cast(bf16x8, m[j]), d[c][kt]);
                }
#pragma unroll
            for (int kt = 0; kt < 2; ++kt) *(f32x4*)(sS + (wave * 16 + r16) * SXR + 16 * kt + 4 * kg) = cr ? d[1][kt] : d[0][kt];
        }
        __syncthreads();
        TS(0, T, 3)
        if (wave < 2) {
            f32x4 s = *(const f32x4*)(sS + srow * SXR + q8 * 4);
#pragma unroll
            for (int w = 1; w < 7; ++w) s += *(const f32x4*)(sS + (w * 16 + srow) * SXR + q8 * 4);
            float x[4];
#pragma unroll
            for (int j = 0; j < 4; ++j) x[j] = rb(s[j]);
            float mx = fmaxf(fmaxf(x[0], x[1]), fmaxf(x[2], x[3]));
            mx = fmaxf(mx, dpp_f<0xB1>(mx)); mx = fmaxf(mx, dpp_f<0x4E>(mx)); mx = fmaxf(mx, dpp_f<0x141>(mx));
            const float mb = -mx * SC_L2E;
            float sum = 0.f;
#pragma unroll
            for (int j = 0; j < 4; ++j) { x[j] = __builtin_amdgcn_exp2f(fmaf(x[j], SC_L2E, mb)); sum += x[j]; }
            sum += dpp_f<0xB1>(sum); sum += dpp_f<0x4E>(sum); sum += dpp_f<0x141>(sum);
            const v2u o = {san(pack2(x[0], x[1])), san(pack2(x[2], x[3]))};
            const v2u st = {san(__float_as_uint(mx)), san(__float_as_uint(sum))};
#pragma unroll
            for (int c = 0; c < NCP; ++c) {
                BST64(o, rX, vE, (unsigned)((sl * NCP + c) * XP_SZ));
                if (q8 == 0) BST64(st, rX, vST, (unsigned)((sl * NCP + c) * XP_SZ));
            }
            if (T > 0) {
#pragma unroll
                for (int c = 0; c < NCP; ++c) {
                    BST64(sent2, rX, vE, (unsigned)((spv * NCP + c) * XP_SZ));
                    if (q8 == 0) BST64(sent2, rX, vST, (unsigned)((spv * NCP + c) * XP_SZ));
                }
            }
        }
        TS(0, T, 4)
        if (wave >= 2) SLP(dl[6], DKL)
        if (wave < 7 && T + 1 < nticks - 1) load_k(T + 1);
        TS(0, T, 5)
    }
}

DI void sm_role(const u16* __restrict__ mem0, char* ws, char* smem, int h, int p, int lag, int nticks, unsigned long long pc DLPARAM) {
    asm volatile("" : "+s"(ws));
    const unsigned cpf = code_touch(pc, (h & 1) * NSP + p, 8);
    SLP(dl[4], DW0)
    asm volatile("" :: "v"(cpf) : "memory");
    u16* sMem = (u16*)smem;
    float* sS = (float*)(sMem + 16 * MS);
    const int tid = threadIdx.x, lane = tid & 63, r16 = lane & 15, kg = lane >> 4, cr = r16 >> 3;
    const int wave = __builtin_amdgcn_readfirstlane(tid >> 6);
    const int wq = wave < 7 ? wave : 0;
    const u16* WA = (const u16*)(ws + OFF_WAB);
    auto rX = __builtin_amdgcn_make_buffer_rsrc(ws + OFF_XC, 0, (unsigned)(OFF_FL - OFF_XC), 0x00020000);
    constexpr unsigned XP0 = (unsigned)(OFF_XP - OFF_XC);
    const v4u sent4 = {SENT, SENT, SENT, SENT};
    bf16x8 wreg[NPR][2][2];
    {
        const int nrow = 8 * (r16 >> 2) + (r16 & 3);
#pragma unroll
        for (int q = 0; q < NPR; ++q) {
            const int gp = p * NPR + q;
#pragma unroll
            for (int t = 0; t < 2; ++t)
#pragma unroll
                for (int j = 0; j < 2; ++j) {
                    const bf16x8 z = {0, 0, 0, 0, 0, 0, 0, 0};
                    wreg[q][t][j] = z;
                    if (gp < 14) wreg[q][t][j] = *(const bf16x8*)(WA + wab_off(h * NPART, 32 * gp + nrow + 4 * t, (2 * wq + j) * 32 + kg * 8));
                }
        }
    }
    const unsigned vS = XP0 + XP_SM + (unsigned)(((h * NSP + p) * 16) * 32 + tid * 16);
    const int ci = xcd_of(blockIdx.x);
    for (int T = 0; T < nticks - 1; ++T) {
        const int sl = T & (RING - 1), spv = (T - 1) & (RING - 1);
        const int tc0 = T, tc1 = T - lag;
        const bool act0 = tc0 < NSEG_, act1 = tc1 >= 0 && tc1 < NSEG_;
        f32x4 a[NPR][2];
        TS(2, T, 0)
        if (wave < 7) {
            SLP(dl[5], DSM)
            v4u m[2];
            get_frag(mem0, ws, rX, (unsigned)((sl * NCC + (ci % NCC)) * XC_SZ), T, wave, r16, kg, cr ? act1 : act0, cr ? tc1 : tc0, m);
            TS(2, T, 1)
            *(v4u*)(sMem + r16 * MS + 64 * wave + 8 * kg) = m[0];
            *(v4u*)(sMem + r16 * MS + 64 * wave + 32 + 8 * kg) = m[1];
#pragma unroll
            for (int q = 0; q < NPR; ++q)
#pragma unroll
                for (int t = 0; t < 2; ++t) {
                    a[q][t] = (f32x4){0.f, 0.f, 0.f, 0.f};
                    a[q][t] = MFMA16(wreg[q][t][0], __builtin_bit_cast(bf16x8, m[0]), a[q][t]);
                }
#pragma unroll
            for (int q = 0; q < NPR; ++q)
#pragma unroll
                for (int t = 0; t < 2; ++t) a[q][t] = MFMA16(wreg[q][t][1], __builtin_bit_cast(bf16x8, m[1]), a[q][t]);
        }
        __syncthreads();
        TS(2, T, 2)
        if (wave < 7) {
            f32x4 sc0 = {0.f, 0.f, 0.f, 0.f}, sc1 = sc0;
#pragma unroll
            for (int q = 0; q < NPR; ++q) {
                const int gp = p * NPR + q < 14 ? p * NPR + q : 13;
                const bf16x8 kf = *(const bf16x8*)(sMem + r16 * MS + 32 * gp + 8 * kg);
                const v4u b = {pack2(a[q][0][0], a[q][0][1]), pack2(a[q][0][2], a[q][0][3]), pack2(a[q][1][0], a[q][1][1]), pack2(a[q][1][2], a[q][1][3])};
                if (q & 1) sc1 = MFMA16(kf, __builtin_bit_cast(bf16x8, b), sc1);
                else sc0 = MFMA16(kf, __builtin_bit_cast(bf16x8, b), sc0);
            }
            sc0 += sc1;
            if ((kg >> 1) == cr) *(f32x4*)(sS + (wave * 16 + r16) * 8 + (kg & 1) * 4) = sc0;
        }
        __syncthreads();
        TS(2, T, 3)
        if (tid < 32) {
            f32x4 s = *(const f32x4*)(sS + tid * 4);
#pragma unroll
            for (int w = 1; w < 7; ++w) s += *(const f32x4*)(sS + w * 128 + tid * 4);
            const v4u o = {san(__float_as_uint(s[0])), san(__float_as_uint(s[1])), san(__float_as_uint(s[2])), san(__float_as_uint(s[3]))};
#pragma unroll
            for (int c = 0; c < NCP; ++c) BST(o, rX, vS, (unsigned)((sl * NCP + c) * XP_SZ));
            if (T > 0) {
#pragma unroll
                for (int c = 0; c < NCP; ++c) BST(sent4, rX, vS, (unsigned)((spv * NCP + c) * XP_SZ));
            }
        }
        TS(2, T, 4)
    }
}

DI void value_role(const u16* __restrict__ mem0, char* ws, char* smem, int v, int lag, int nticks, unsigned long long pc DLPARAM) {
    asm volatile("" : "+s"(ws));
    const unsigned cpf = ((v & 2) == 0 || v >= 26) ? code_touch(pc, 6 + (v & 1), 8) : 0u;
    SLP(dl[4], DW0)
    asm volatile("" :: "v"(cpf) : "memory");
    u16* sMem = (u16*)smem;
    float* sRd = (float*)(sMem + 16 * MS);
    const int tid = threadIdx.x, lane = tid & 63;
    const int wave = __builtin_amdgcn_readfirstlane(tid >> 6);
    const int r16 = lane & 15, kg = lane >> 4;
    const int cr = r16 >> 3;
    const int ha = 2 * wave, hb = ha + 1;
    const u16* WB = (const u16*)(ws + OFF_WAB);
    auto rX = __builtin_amdgcn_make_buffer_rsrc(ws + OFF_XC, 0, (unsigned)(OFF_FL - OFF_XC), 0x00020000);
    auto rU = __builtin_amdgcn_make_buffer_rsrc(ws + OFF_U, 0, (unsigned)(OFF_K - OFF_U), 0x00020000);
    auto rM = __builtin_amdgcn_make_buffer_rsrc(ws + OFF_MEMH, 0, (unsigned)(OFF_PART - OFF_MEMH), 0x00020000);
    constexpr unsigned XP0 = (unsigned)(OFF_XP - OFF_XC), WM0 = (unsigned)(OFF_WM - OFF_MEMH);
    bf16x8 wreg[28];
#pragma unroll
    for (int ks = 0; ks < 14; ++ks) {
        wreg[ks] = *(const bf16x8*)(WB + wab_off(ha * NPART + 1, 16 * v + r16, ks * 32 + kg * 8));
        wreg[14 + ks] = *(const bf16x8*)(WB + wab_off(hb * NPART + 1, 16 * v + r16, ks * 32 + kg * 8));
    }
    const v4u zero4 = {0u, 0u, 0u, 0u};
    const v2u sent2 = {SENT, SENT};
    const unsigned vU = (unsigned)(((v * 4 + kg) * 16 + r16) * 16);
#define UEP 0
    v4u uf[4];
    auto load_u = [&](int T, bool blocking, bool ok) {
        const int t0 = T, t1 = T - lag;
        const bool a0 = ok && t0 < NSEG_, a1 = ok && t1 >= 0 && t1 < NSEG_;
        if (blocking) {
            if (a0) spin_flag<4, PFS>(ws, FL_PROJ + t0 * PFB, NP_);
            if (a1) spin_flag<4, PFS>(ws, FL_PROJ + (64 + t1) * PFB, NP_);
            VMWAIT();
        }
        const unsigned v0 = a0 ? vU : 0x80000000u, v1 = a1 ? vU : 0x80000000u;
        const unsigned s0 = (unsigned)((a0 ? t0 : 0) * 16 + ha) * RU_BLK, s1 = (unsigned)((64 + (a1 ? t1 : 0)) * 16 + ha) * RU_BLK;
        uf[0] = BLD(rU, v0, s0); uf[2] = BLD(rU, v0, s0 + RU_BLK);
        uf[1] = BLD(rU, v1, s1); uf[3] = BLD(rU, v1, s1 + RU_BLK);
    };
    auto flags_u = [&](int T, unsigned (&f)[4]) {
        const int t0 = T, t1 = T - lag;
        const bool a0 = t0 < NSEG_, a1 = t1 >= 0 && t1 < NSEG_;
        f[0] = 1u; f[1] = 1u; f[2] = 1u; f[3] = 1u;
        if (a0) { f[0] = flag_load(ws, FL_PROJ + t0 * PFB + lane * PFS); if (lane + 64 < NP_) f[1] = flag_load(ws, FL_PROJ + t0 * PFB + 64 * PFS + lane * PFS); }
        if (a1) { f[2] = flag_load(ws, FL_PROJ + (64 + t1) * PFB + lane * PFS); if (lane + 64 < NP_) f[3] = flag_load(ws, FL_PROJ + (64 + t1) * PFB + 64 * PFS + lane * PFS); }
    };
    const bool mvalid = (kg >> 1) == cr;
    const int hs = mvalid ? ha : hb;
    const unsigned vPe = XP0 + XP_E + (unsigned)((ha * 16 + r16) * 64 + kg * 16);
    const unsigned vPst = XP0 + XP_ST + (unsigned)((hs * 16 + r16) * 8);
    const unsigned vPs = XP0 + XP_SM + (unsigned)(((hs * NSP) * 16 + r16) * 32 + (kg & 1) * 16);
    const int pcopy = xcd_of(blockIdx.x) % NCP;
    const unsigned vC = (unsigned)(v * 512 + lane * 8);
    const unsigned vWM = WM0 + (unsigned)((16 * v + r16) * 16 + (kg & 1) * 8);
    const int ci = xcd_of(blockIdx.x);
    bool have_u = false;
    auto set_flags = [&](int T) {
        const int c0 = T, c1 = T - lag;
        if (tid < NG0) { if (c0 < NSEG_) flag_set(ws, FL_MEM + c0 * 1024 + tid * 128 + v * 4); }
        else if (tid >= 8 && tid < 8 + NG1) { if (c1 >= 0 && c1 < NSEG_) flag_set(ws, FL_MEM + (64 + c1) * 1024 + (tid - 8) * 128 + v * 4); }
    };

    wait_flag<PFS>(ws, FL_PROJ, NP_);
    load_u(0, false, true); have_u = true;
    __builtin_amdgcn_s_waitcnt(0);
    for (int T = 0; T < nticks; ++T) {
        const int sl = T & (RING - 1), sn = (T + 1) & (RING - 1), spv = (T - 1) & (RING - 1);
        const int tc0 = T, tc1 = T - lag;
        const bool act0 = tc0 < NSEG_, act1 = tc1 >= 0 && tc1 < NSEG_;
        const bool more = T + 1 < nticks;
        TS(1, T, 0)
        SLP(dl[1], DVC)
        get_mem(mem0, ws, rX, sMem, (unsigned)((sl * NCC + (ci % NCC)) * XC_SZ), T, tid, act0, act1, tc0, tc1);
        VMWAIT();
        __syncthreads();
        TS(1, T, 1)
        if (T > 0) set_flags(T - 1);
        unsigned fnext[4] = {1u, 1u, 1u, 1u};
        if (more) flags_u(T + 1, fnext);
        __builtin_amdgcn_sched_barrier(0);
        f32x4 wa = {0.f, 0.f, 0.f, 0.f}, wb = wa;
#pragma unroll
        for (int ks = 0; ks < 14; ++ks) {
            const bf16x8 m = *(const bf16x8*)(sMem + r16 * MS + ks * 32 + kg * 8);
            wa = MFMA16(m, wreg[ks], wa);
            wb = MFMA16(m, wreg[14 + ks], wb);
        }
        const v4u mh = *(const v4u*)(sMem + ((lane >> 1) & 15) * MS + 16 * v + (lane & 1) * 8);
        const v4u wf = {pack2(wa[0], wa[1]), pack2(wa[2], wa[3]), pack2(wb[0], wb[1]), pack2(wb[2], wb[3])};
        auto helper_stores = [&]() {
            const int ck = kg >> 1;
            const int seg = ck ? 64 + tc1 : tc0;
            if (ck ? act1 : act0) {
                const v2u wA = {wf.x, wf.y}, wB = {wf.z, wf.w};
                BST64(wA, rM, vWM + (unsigned)seg * (16u * 7168u), (unsigned)ha * 7168u);
                BST64(wB, rM, vWM + (unsigned)seg * (16u * 7168u), (unsigned)hb * 7168u);
            }
            if (wave == 0 && lane < 32) {
                const int row = lane >> 1, c = row >> 3;
                if (c ? act1 : act0) BST(mh, rM, (unsigned)(v * 256 + (row & 7) * 32 + (lane & 1) * 16) + (unsigned)(c ? 64 + tc1 : tc0) * 7168u, 0u);
            }
            if (T > 1) {
#pragma unroll
                for (int c = 0; c < NCC; c += 8) if (wave + c < NCC) BST64(sent2, rX, vC, (unsigned)((spv * NCC + wave + c) * XC_SZ));
            }
        };
        TS(1, T, 2)
        if (more) {
            SLP(dl[2], DVP)
            const unsigned sP = (unsigned)((sl * NCP + pcopy) * XP_SZ);
            struct PS { v4u ea, eb, sm[NSP]; v2u st; };
            auto issue = [&](PS& q) {
                asm volatile("" ::: "memory");
                q.ea = BLD(rX, vPe, sP); q.eb = BLD(rX, vPe + 1024, sP);
                q.st = BLD64(rX, vPst, sP);
#pragma unroll
                for (int p = 0; p < NSP; ++p) q.sm[p] = BLD(rX, vPs + 512 * p, sP);
            };
            auto okp = [&](const PS& q) -> bool {
                unsigned bad = (q.ea.x == SENT) | (q.ea.y == SENT) | (q.ea.z == SENT) | (q.ea.w == SENT) | (q.eb.x == SENT) | (q.eb.y == SENT) | (q.eb.z == SENT) | (q.eb.w == SENT) | (q.st.x == SENT) | (q.st.y == SENT);
#pragma unroll
                for (int p = 0; p < NSP; ++p) bad |= (q.sm[p].x == SENT) | (q.sm[p].y == SENT) | (q.sm[p].z == SENT) | (q.sm[p].w == SENT);
                return __builtin_amdgcn_ballot_w64(bad != 0u) == 0ull;
            };
            f32x4 d;
            auto finish = [&](const PS& q) {
                TS(1, T, 3)
                const f32x4 z4 = {0.f, 0.f, 0.f, 0.f};
                const f32x4 da0 = MFMA16(__builtin_bit_cast(bf16x8, uf[0]), __builtin_bit_cast(bf16x8, q.ea), z4);
                const f32x4 da1 = MFMA16(__builtin_bit_cast(bf16x8, uf[1]), __builtin_bit_cast(bf16x8, q.ea), z4);
                const f32x4 db0 = MFMA16(__builtin_bit_cast(bf16x8, uf[2]), __builtin_bit_cast(bf16x8, q.eb), z4);
                const f32x4 db1 = MFMA16(__builtin_bit_cast(bf16x8, uf[3]), __builtin_bit_cast(bf16x8, q.eb), z4);
                f32x4 s4 = __builtin_bit_cast(f32x4, q.sm[0]);
#pragma unroll
                for (int p = 1; p < NSP; ++p) s4 += __builtin_bit_cast(f32x4, q.sm[p]);
                float x[4];
#pragma unroll
                for (int j = 0; j < 4; ++j) x[j] = rb(s4[j]);
                const float mxx = __uint_as_float(q.st.x), sxx = __uint_as_float(q.st.y);
                float mx = fmaxf(fmaxf(fmaxf(x[0], x[1]), fmaxf(x[2], x[3])), mxx);
                { auto r = __builtin_amdgcn_permlane16_swap(__float_as_uint(mx), __float_as_uint(mx), false, false); mx = fmaxf(__uint_as_float(r[0]), __uint_as_float(r[1])); }
                const float mb = -mx * SC_L2E;
                float sum = 0.f;
#pragma unroll
                for (int j = 0; j < 4; ++j) { x[j] = __builtin_amdgcn_exp2f(fmaf(x[j], SC_L2E, mb)); sum += x[j]; }
                const float fx = __builtin_amdgcn_exp2f(fmaf(mxx, SC_L2E, mb));
                { auto r = __builtin_amdgcn_permlane16_swap(__float_as_uint(sum), __float_as_uint(sum), false, false); sum = __uint_as_float(r[0]) + __uint_as_float(r[1]); }
                const float inv = __builtin_amdgcn_rcpf(fmaf(fx, sxx, sum));
                const auto rg = __builtin_amdgcn_permlane32_swap(__float_as_uint(fx * inv), __float_as_uint(fx * inv), false, false);
                const unsigned p0 = pack2(x[0] * inv, x[1] * inv), p1 = pack2(x[2] * inv, x[3] * inv);
                const auto r0 = __builtin_amdgcn_permlane32_swap(p0, p0, false, false);
                const auto r1 = __builtin_amdgcn_permlane32_swap(p1, p1, false, false);
                const float ga = __uint_as_float(cr ? rg[1] : rg[0]), gb = __uint_as_float(cr ? rg[0] : rg[1]);
                const unsigned q0 = cr ? r0[1] : r0[0], q1 = cr ? r1[1] : r1[0], q2 = cr ? r0[0] : r0[1], q3 = cr ? r1[0] : r1[1];
                const v4u pm = {mvalid ? q0 : 0u, mvalid ? q1 : 0u, mvalid ? q2 : 0u, mvalid ? q3 : 0u};
                d = MFMA16(__builtin_bit_cast(bf16x8, wf), __builtin_bit_cast(bf16x8, pm), z4);
#pragma unroll
                for (int j = 0; j < 4; ++j) d[j] += ga * (cr ? da1[j] : da0[j]) + gb * (cr ? db1[j] : db0[j]);
            };
            {
                PS qa;
                for (int sp = 0;; ++sp) {
                    issue(qa);
                    __builtin_amdgcn_s_sleep(2);
                    if (okp(qa)) break;
                    if (sp == SPIN_CAP) { set_err(ws, 2u); break; }
                }
                VMWAIT();
                finish(qa);
            }
            *(f32x4*)(sRd + (wave * 16 + r16) * 16 + kg * 4) = d;
            __syncthreads();
            f32x4 s = *(const f32x4*)(sRd + lane * 4);
#pragma unroll
            for (int w = 1; w < 8; ++w) s += *(const f32x4*)(sRd + w * 256 + lane * 4);
            const v2u o = {san(pack2(s[0], s[1])), san(pack2(s[2], s[3]))};
#pragma unroll
            for (int c = 0; c < NCC; c += 8) if (wave + c < NCC) BST64(o, rX, vC, (unsigned)((sn * NCC + wave + c) * XC_SZ));
            TS(1, T, 5)
            __builtin_amdgcn_sched_barrier(0);
            helper_stores();
            asm volatile("" : "+v"(fnext[0]), "+v"(fnext[1]), "+v"(fnext[2]), "+v"(fnext[3]));
            have_u = __builtin_amdgcn_ballot_w64((fnext[0] & fnext[1] & fnext[2] & fnext[3]) == 0u) == 0ull;
            load_u(T + 1, !have_u, T + 2 < nticks);
            TS(1, T, 4)
        } else {
            load_u(T + 1, false, false);
            helper_stores();
            VMWAIT();
            __syncthreads();
            set_flags(T);
        }
    }
}

DI void pmap(int cb, int wave, int& h, int& part, int& g) {
    const bool lo = cb < 60;
    const int x = lo ? cb / 10 : 6 + (cb - 60) / 12;
    const int j = (lo ? cb % 10 : (cb - 60) % 12) * 8 + wave;
    const int n1 = lo ? 60 : 44;
    if (j < 28) { part = 0; h = 2 * x + j / 14; g = j % 14; }
    else if (j < n1) { part = 1; g = lo ? 2 * x + (j - 28) / 16 : 6 + x; h = (j - 28) % 16; }
    else { const int q = lo ? 20 * x + j - 60 : 120 + 52 * (x - 6) + j - 44; part = 2; h = q / 14; g = q % 14; }
}
#define PLM 3
DI int logical_wg(int wg) {
    const int x = xcd_of(wg), i = wg >> 3;
    if (x == 0) return i < 28 ? WG_V + i : 252 + (i - 28);
    if (x < 3) return 2 * i + (x - 1);
    return 92 + (x - 3) + 5 * i;
}
static_assert(NSP == 3 && WG_A0 == 176, "placement tables");
template <bool isU>
DI void proj_role(char* ws, char* smem, int cb) {
    asm volatile("" : "+s"(ws));
    u16* sH = (u16*)smem;
    const int tid = threadIdx.x, lane = tid & 63;
    const int wave = __builtin_amdgcn_readfirstlane(tid >> 6);
    const int r = lane & 31, hh = lane >> 5;
    const int pr = (r & 19) | ((r & 4) << 1) | ((r & 8) >> 1);
    int h, part, g; pmap(cb, wave, h, part, g);
    const u16* WT = (const u16*)(ws + OFF_WAB);
    auto rH = __builtin_amdgcn_make_buffer_rsrc(ws + OFF_HB, 0, 2 * S_ * D_ * 2, 0x00020000);
    auto rO = __builtin_amdgcn_make_buffer_rsrc(ws + (part == 0 ? OFF_R : part == 1 ? OFF_U : OFF_K), 0, 2 * 64 * 16 * RU_BLK, 0x00020000);
    bf16x8 fw[28];
#pragma unroll
    for (int ks = 0; ks < 28; ++ks) fw[ks] = *(const bf16x8*)(WT + wab_off(h * NPART + part, 32 * g + (isU ? r : pr), ks * 16 + hh * 8));
    const int hrow = isU ? pr : r;
    const unsigned vo = isU ? (unsigned)((((2 * g + (r >> 4)) * 4 + hh) * 16 + (r & 15)) * 16) : (unsigned)((((2 * g) * 2 + hh) * 32 + r) * 16);
    const unsigned so1 = isU ? 2 * 16 * 16 : 2 * 32 * 16;
    u16* sHB = sH + 32 * MS;
    auto ld_rows = [&](int ls, v4u (&ph)[4]) {
#pragma unroll
        for (int i = 0; i < 4; ++i) {
            const int c = tid + 512 * i;
            ph[i] = (v4u){0u, 0u, 0u, 0u};
            if (c < 32 * 56) ph[i] = __builtin_amdgcn_raw_buffer_load_b128(rH, c * 16, (unsigned)(ls * 32 * D_ * 2), 0);
        }
    };
    auto ld_rows1 = [&](int ls, v4u (&ph)[4]) {
#pragma unroll
        for (int i = 0; i < 4; ++i) {
            const int c = tid + 512 * i;
            ph[i] = (v4u){0u, 0u, 0u, 0u};
            if (c < 32 * 56) ph[i] = __builtin_amdgcn_raw_buffer_load_b128(rH, c * 16, (unsigned)(ls * 32 * D_ * 2), HAUX);
        }
    };
    auto st_rows = [&](u16* buf, const v4u (&ph)[4]) {
#pragma unroll
        for (int i = 0; i < 4; ++i) {
            const int c = tid + 512 * i;
            if (c < 32 * 56) *(v4u*)(buf + (c / 56) * MS + (c % 56) * 8) = ph[i];
        }
    };
    auto gemm = [&](const u16* buf, int ls) {
        f32x16 acc;
#pragma unroll
        for (int i = 0; i < 16; ++i) acc[i] = 0.f;
#pragma unroll
        for (int ks = 0; ks < 28; ++ks) {
            const bf16x8 fh = *(const bf16x8*)(buf + hrow * MS + ks * 16 + hh * 8);
            acc = isU ? MFMA32(fh, fw[ks], acc) : MFMA32(fw[ks], fh, acc);
        }
        const unsigned so = (unsigned)(ls * 16 + h) * RU_BLK;
#pragma unroll
        for (int a = 0; a < 2; ++a) {
            v4u o = {pack2(acc[8 * a], acc[8 * a + 1]), pack2(acc[8 * a + 2], acc[8 * a + 3]), pack2(acc[8 * a + 4], acc[8 * a + 5]), pack2(acc[8 * a + 6], acc[8 * a + 7])};
            BST(o, rO, vo, so + a * so1);
        }
    };
    { v4u ph[4]; ld_rows(0, ph); st_rows(sHB, ph); __syncthreads(); }
    for (int k = 0; k < NSEG_ + PAHEAD; ++k) {
        const bool has0 = k < NSEG_, has1 = k >= PAHEAD, hasn = k + 1 < NSEG_;
        const int j1 = k - PAHEAD;
        TS(5, k, 0)
        if (has1) wait_flag(ws, FL_RED + j1 * 128, RW0);
        TS(5, k, 1)
        v4u ph1[4], ph0[4];
        if (has1) ld_rows1(64 + j1, ph1);
        if (hasn) ld_rows(k + 1, ph0);
        if (has0) gemm(sHB, k);
        if (has1) st_rows(sH, ph1);
        __syncthreads();
        TS(5, k, 2)
        if (hasn) st_rows(sHB, ph0);
        if (has1) gemm(sH, 64 + j1);
        TS(5, k, 3)
        VMWAIT();
        __syncthreads();
        if (tid == 0 && has0) flag_set(ws, FL_PROJ + k * PFB + cb * PFS);
        if (tid == 64 && has1) flag_set(ws, FL_PROJ + (64 + j1) * PFB + cb * PFS);
        TS(5, k, 4)
    }
}

template <int HPT>
DI void att_role(char* ws_, char* smem, int layer, int hg, int ph, int nph) {
    constexpr int WPH = 8 / HPT, KPW = 28 / WPH;
    float* sS = (float*)smem;
    u16* sP = (u16*)(sS + 8 * 32 * SSR);
    const v4u zero4 = {0u, 0u, 0u, 0u};
    for (int seg = ph; seg < NSEG_; seg += nph) {
        char* ws = ws_; asm volatile("" : "+s"(ws));
        int tid = threadIdx.x; asm volatile("" : "+v"(tid));
        const int lane = tid & 63;
        const int wave = __builtin_amdgcn_readfirstlane(tid >> 6);
        const int r = lane & 31, hh = lane >> 5;
        const int hl = wave / WPH, k0 = (wave % WPH) * KPW;
        const size_t sh = ((size_t)layer * NSEG_ + seg) * 16 + hg * HPT;
        auto rR = __builtin_amdgcn_make_buffer_rsrc(ws + OFF_R + sh * RU_BLK, 0, HPT * RU_BLK, 0x00020000);
        auto rU = __builtin_amdgcn_make_buffer_rsrc(ws + OFF_U + sh * RU_BLK, 0, HPT * RU_BLK, 0x00020000);
        auto rH = __builtin_amdgcn_make_buffer_rsrc(ws + OFF_HB + ((size_t)layer * S_ + seg * 32) * D_ * 2, 0, 32 * D_ * 2, 0x00020000);
        auto rM = __builtin_amdgcn_make_buffer_rsrc(ws + OFF_MEMH + ((size_t)layer * NSEG_ + seg) * 7168, 0, 7168, 0x00020000);
        auto rW = __builtin_amdgcn_make_buffer_rsrc(ws + OFF_WM + sh * 7168, 0, HPT * 7168, 0x00020000);
        TS(3, seg, 0)
        wait_flag<PFS>(ws, FL_PROJ + (layer * 64 + seg) * PFB, NP_);
        TS(3, seg, 1)
        v4u rf[KPW], uf[HPT][2][2];
        f32x16 s1, s0;
#pragma unroll
        for (int i = 0; i < 16; ++i) { s1[i] = 0.f; s0[i] = 0.f; }
        {
            v4u kf[KPW];
#pragma unroll
            for (int q = 0; q < KPW; ++q) {
                const int ks = k0 + q;
                rf[q] = BLD(rR, ((ks * 2 + hh) * 32 + r) * 16, (unsigned)hl * RU_BLK);
                kf[q] = __builtin_amdgcn_raw_buffer_load_b128(rH, (r * D_ + ks * 16 + hh * 8) * 2, 0, HPT == HPT0 ? 0 : KAUX);
            }
#pragma unroll
            for (int q = 0; q < KPW; ++q) s1 = MFMA32(__builtin_bit_cast(bf16x8, kf[q]), __builtin_bit_cast(bf16x8, rf[q]), s1);
        }
#pragma unroll
        for (int g4 = 0; g4 < 4; ++g4) {
            f32x4 v = {s1[4 * g4], s1[4 * g4 + 1], s1[4 * g4 + 2], s1[4 * g4 + 3]};
            *(f32x4*)(sS + (wave * 32 + r) * SSR + 8 + 8 * g4 + 4 * hh) = v;
        }
#pragma unroll
        for (int x = 0; x < HPT; ++x)
#pragma unroll
            for (int tl = 0; tl < 2; ++tl) {
                const int dt = wave + 8 * tl;
#pragma unroll
                for (int s = 0; s < 2; ++s) {
                    uf[x][tl][s] = zero4;
                    if (dt < 14) uf[x][tl][s] = BLD(rU, (((2 * dt + (r >> 4)) * 4 + 2 * s + hh) * 16 + (r & 15)) * 16, (unsigned)x * RU_BLK);
                }
            }
        wait_flag(ws, FL_MEM + (layer * 64 + seg) * 1024 + hg * 128, 28);
        TS(3, seg, 2)
        v4u wm[HPT][2];
        {
            v4u mf[KPW];
#pragma unroll
            for (int q = 0; q < KPW; ++q) {
                mf[q] = zero4;
                if (r < 8) mf[q] = BLD(rM, (k0 + q) * 256 + r * 32 + hh * 16, 0);
            }
#pragma unroll
            for (int x = 0; x < HPT; ++x)
#pragma unroll
                for (int tl = 0; tl < 2; ++tl) {
                    const int dt = wave + 8 * tl;
                    wm[x][tl] = zero4;
                    if (dt < 14 && hh == 0) wm[x][tl] = BLD(rW, (dt * 32 + r) * 16, (unsigned)x * 7168u);
                }
#pragma unroll
            for (int q = 0; q < KPW; ++q) s0 = MFMA32(__builtin_bit_cast(bf16x8, mf[q]), __builtin_bit_cast(bf16x8, rf[q]), s0);
        }
        { f32x4 v = {s0[0], s0[1], s0[2], s0[3]}; *(f32x4*)(sS + (wave * 32 + r) * SSR + 4 * hh) = v; }
        __syncthreads();
#pragma unroll
        for (int x = 0; x < HPT; ++x) {
            const int q = tid >> 4, c16 = tid & 15;
            const bool on = c16 < 10;
            const int c4 = on ? c16 : 0;
            f32x4 s = *(const f32x4*)(sS + (x * WPH * 32 + q) * SSR + c4 * 4);
#pragma unroll
            for (int w = 1; w < WPH; ++w) s += *(const f32x4*)(sS + ((x * WPH + w) * 32 + q) * SSR + c4 * 4);
            float xx[4];
            float mx = -3.0e38f;
#pragma unroll
            for (int j = 0; j < 4; ++j) {
                float v = rb(s[j]) * 0.0625f;
                v = (4 * c4 - 8 + j <= q) ? v : -1000000000.0f;
                xx[j] = on ? v : -3.0e38f; mx = fmaxf(mx, xx[j]);
            }
            mx = rowmax16(mx);
            float sum = 0.f;
#pragma unroll
            for (int j = 0; j < 4; ++j) { xx[j] = on ? __expf(xx[j] - mx) : 0.f; sum += xx[j]; }
            sum = rowsum16(sum);
            const float inv = 1.0f / sum;
            const v2u o = {pack2(xx[0] * inv, xx[1] * inv), pack2(xx[2] * inv, xx[3] * inv)};
            if (on) *(v2u*)(sP + (x * 32 + q) * 48 + c4 * 4) = o;
        }
        __syncthreads();
        auto rP = __builtin_amdgcn_make_buffer_rsrc(ws + OFF_PART + ((size_t)hg * S_ + seg * 32) * D_ * 4, 0, 32 * D_ * 4, 0x00020000);
        {
            f32x16 o[2];
#pragma unroll
            for (int i = 0; i < 16; ++i) { o[0][i] = 0.f; o[1][i] = 0.f; }
#pragma unroll
            for (int x = 0; x < HPT; ++x) {
                bf16x8 pf[3];
#pragma unroll
                for (int s = 0; s < 2; ++s) pf[s] = *(const bf16x8*)(sP + (x * 32 + r) * 48 + 8 + 16 * s + 8 * hh);
                pf[2] = __builtin_bit_cast(bf16x8, hh == 0 ? *(const v4u*)(sP + (x * 32 + r) * 48) : zero4);
#pragma unroll
                for (int tl = 0; tl < 2; ++tl) {
                    if (wave + 8 * tl < 14) {
                        o[tl] = MFMA32(pf[0], __builtin_bit_cast(bf16x8, uf[x][tl][0]), o[tl]);
                        o[tl] = MFMA32(pf[1], __builtin_bit_cast(bf16x8, uf[x][tl][1]), o[tl]);
                        o[tl] = MFMA32(pf[2], __builtin_bit_cast(bf16x8, wm[x][tl]), o[tl]);
                    }
                }
            }
#pragma unroll
            for (int tl = 0; tl < 2; ++tl) {
                const int dt = wave + 8 * tl;
                if (dt < 14) {
#pragma unroll
                    for (int reg = 0; reg < 16; ++reg) {
                        const int t = (reg & 3) + 8 * (reg >> 2) + 4 * hh;
                        __builtin_amdgcn_raw_buffer_store_b32(__float_as_uint(o[tl][reg]), rP, (t * D_ + dt * 32 + r) * 4, 0, 16);
                    }
                }
            }
        }
        TS(3, seg, 3)
        signal_flag(ws, FL_ATT + (layer * 64 + seg) * 128 + hg * 4);
        TS(3, seg, 4)
    }
}

template <int NP>
DI void task_red(char* ws, u16* __restrict__ out, int layer, int trow, int tid) {
    const int lane = tid & 63, wave = tid >> 6;
    const bool on = lane < 56;
    const int lc = on ? lane : 0;
    auto rP = __builtin_amdgcn_make_buffer_rsrc(ws + OFF_PART, 0, (unsigned)((size_t)8 * S_ * D_ * 4), 0x00020000);
    auto rX = __builtin_amdgcn_make_buffer_rsrc(ws + (layer == 0 ? OFF_X1 : OFF_X3), 0, S_ * D_ * 2, 0x00020000);
    auto rX3 = __builtin_amdgcn_make_buffer_rsrc(ws + OFF_X3, 0, S_ * D_ * 2, 0x00020000);
    auto rH3 = __builtin_amdgcn_make_buffer_rsrc(ws + OFF_HB + (size_t)S_ * D_ * 2, 0, S_ * D_ * 2, 0x00020000);
    const u16* yr = (const u16*)(ws + OFF_Y);
    const int t = trow;
    f32x4 pa[NP], pb[NP];
#pragma unroll
    for (int h = 0; h < NP; ++h) {
        pa[h] = __builtin_bit_cast(f32x4, __builtin_amdgcn_raw_buffer_load_b128(rP, ((h * S_ + t) * D_ + lc * 8) * 4, 0, 16));
        pb[h] = __builtin_bit_cast(f32x4, __builtin_amdgcn_raw_buffer_load_b128(rP, ((h * S_ + t) * D_ + lc * 8 + 4) * 4, 0, 16));
    }
    v4u xw = __builtin_amdgcn_raw_buffer_load_b128(rX, (t * D_ + lc * 8) * 2, 0, 16);
    v4u yw = *(const v4u*)(yr + (size_t)t * D_ + lc * 8);
    float x[8], ln[8], y[8];
    unpack8(xw, x); unpack8(yw, y);
#pragma unroll
    for (int i = 0; i < 4; ++i) {
        float sa = 0.f, sb = 0.f;
#pragma unroll
        for (int h = 0; h < NP; ++h) { sa += pa[h][i]; sb += pb[h][i]; }
        x[i] = on ? rb(x[i] + rb(sa)) : 0.f; x[4 + i] = on ? rb(x[4 + i] + rb(sb)) : 0.f;
    }
    if (layer == 1) {
        if (on) *(v4u*)(out + (size_t)t * D_ + lane * 8) = pack8(x);
    } else {
        layernorm8(x, ln, on);
#pragma unroll
        for (int i = 0; i < 8; ++i) x[i] = on ? rb(x[i] + rb(ln[i] * y[i])) : 0.f;
        layernorm8(x, ln, on);
        if (on) {
            __builtin_amdgcn_raw_buffer_store_b128(pack8(x), rX3, (t * D_ + lane * 8) * 2, 0, 16);
            __builtin_amdgcn_raw_buffer_store_b128(pack8(ln), rH3, (t * D_ + lane * 8) * 2, 0, 16);
        }
    }
}

#define ABLARG
__global__ void __launch_bounds__(512, 2) k_mega(const u16* __restrict__ mem0, char* __restrict__ ws_, u16* __restrict__ out_ DLARG ABLARG) {
    __shared__ __attribute__((aligned(16))) char smem[LDS_BYTES];
    const int wg = logical_wg(blockIdx.x);
    unsigned long long pc; asm volatile("s_getpc_b64 %0" : "=s"(pc));
    TSINIT()
    if (wg < WG_SM) { sx_role(mem0, ws_, smem, wg, LAG, NSEG_ + LAG DLPASS); TSDUMP(ws_) return; }
    if (wg < WG_V) { const int k = wg - WG_SM; sm_role(mem0, ws_, smem, k / NSP, k % NSP, LAG, NSEG_ + LAG, pc DLPASS); TSDUMP(ws_) return; }
    if (wg < WG_P) { value_role(mem0, ws_, smem, wg - WG_V, LAG, NSEG_ + LAG, pc DLPASS); TSDUMP(ws_) return; }
    if (wg < WG_A0) {
        const int cb = wg - WG_P;
        int h, part, g; pmap(cb, __builtin_amdgcn_readfirstlane(threadIdx.x >> 6), h, part, g);
        if (part == 1) proj_role<true>(ws_, smem, cb); else proj_role<false>(ws_, smem, cb);
        TSDUMP(ws_) return;
    }
    if (wg < WG_A1) { const int k = wg - WG_A0; att_role<HPT0>(ws_, smem, 0, k % NG0, k / NG0, NA0); TSDUMP(ws_) return; }
    if (wg < WG_R0) { const int k = wg - WG_A1; att_role<HPT1>(ws_, smem, 1, k % NG1, k / NG1, NA1); TSDUMP(ws_) return; }
    {
        const int layer = wg >= WG_R1, k = wg - (layer ? WG_R1 : WG_R0);
        const int nw = layer ? 4 : RW0, nph = layer ? NR1 : NR0;
        const int j = k % nw, ph = k / nw, rows = 32 / nw;
        if (ph >= nph) { TSDUMP(ws_) return; }
        for (int seg = ph; seg < NSEG_; seg += nph) {
            char* ws = ws_; asm volatile("" : "+s"(ws));
            u16* out = out_; asm volatile("" : "+s"(out));
            int tid = threadIdx.x; asm volatile("" : "+v"(tid));
            const int wave = __builtin_amdgcn_readfirstlane(tid >> 6);
            TS(4, seg, 0)
            wait_flag(ws, FL_ATT + (layer * 64 + seg) * 128, layer ? NG1 : NG0);
            TS(4, seg, 1)
            if (layer) task_red<NG1>(ws, out, 1, seg * 32 + j * 8 + wave, tid);
            else { if (wave < rows) task_red<NG0>(ws, out, 0, seg * 32 + j * rows + wave, tid); signal_flag(ws, FL_RED + seg * 128 + j * 4); }
            TS(4, seg, 2)
        }
        TSDUMP(ws_)
    }
}

static __device__ __attribute__((noinline, used)) void zz_end() { asm volatile("s_nop 0"); }

extern "C" void kernel_launch(void* const* d_in, const int* in_sizes, int n_in, void* d_out, int out_size, void* d_ws, size_t ws_size, hipStream_t stream) {
    const u16* A_q = (const u16*)d_in[0];
    const u16* A_k = (const u16*)d_in[1];
    const u16* A_v = (const u16*)d_in[2];
    const u16* A_shk = (const u16*)d_in[3];
    const u16* A_ssmk = (const u16*)d_in[4];
    const u16* A_ssmD = (const u16*)d_in[5];
    const u16* B_mem0 = (const u16*)d_in[7];
    const u16* B_Wq = (const u16*)d_in[8];
    const u16* B_Wk = (const u16*)d_in[9];
    const u16* B_Wv = (const u16*)d_in[10];
    const u16* B_Wo = (const u16*)d_in[11];
    char* ws = (char*)d_ws;
    u16* out = (u16*)d_out;

    k_pre1<<<NB_PRE, 512, 0, stream>>>(A_k, A_v, A_shk, B_Wq, B_Wk, B_Wv, B_Wo, ws);
    k_pre2<<<448, 256, 0, stream>>>(A_ssmk, A_ssmD, ws);
    k_stage1<<<256, 512, 0, stream>>>(A_q, ws);
    k_mega<<<256, 512, 0, stream>>>(B_mem0, ws, out);
}
```
